```python
import torch

def _softmax(x, axis=-1):
    return torch.softmax(x, dim=axis)
def _gelu(x):
    return torch.nn.functional.gelu(x, approximate="tanh")
def _silu(x):
    return torch.nn.functional.silu(x)
def _sigmoid(x):
    return torch.sigmoid(x)
def _layernorm(x, axis=-1, eps=1e-5):
    m = x.mean(dim=axis, keepdim=True)
    v = x.var(dim=axis, keepdim=True, unbiased=False)
    return (x - m) / torch.sqrt(v + eps)
def _scatter_add(tgt, idx, src, axis=0):
    return tgt.clone().index_add_(axis, idx, src)
def _segment_sum(src, seg, n):
    out = torch.zeros((n,) + src.shape[1:], dtype=src.dtype)
    return out.index_add_(0, seg, src)
def _pad(x, pw):
    flat = tuple(v for pair in reversed(pw) for v in pair)
    return torch.nn.functional.pad(x, flat)

kernel_name = 'gated_mix__linear_attn_block__dense_activation_squared_relu__prompt'

def setup_inputs():

    _w = (lambda a: (lambda t: t.to(torch.bfloat16) if t.is_floating_point() else t)(torch.as_tensor(a)))
    def _A_setup():
        B, S, D = 1, 2048, 640
        return {
            "x": _w((lambda *s: torch.randn(*s, dtype=torch.bfloat16) * 0.05)(B, S, D)),
            "wqkv": _w((lambda *s: torch.randn(*s, dtype=torch.bfloat16) * 0.8)(D, 3 * D)),
            "wo": _w((lambda *s: torch.randn(*s, dtype=torch.bfloat16) * 0.2)(D, D)),
        }
    _A_in = _A_setup()

    def _B_setup():
        B, S, D, F = 1, 2048, 640, 1536
        return {
            "x": _w((lambda *s: torch.randn(*s, dtype=torch.bfloat16) * 0.05)(B, S, D)),
            "w1": _w((lambda *s: torch.randn(*s, dtype=torch.bfloat16) * 0.1)(D, F)),
            "b1": _w((lambda *s: torch.randn(*s, dtype=torch.bfloat16) * 0.05)(F)),
            "w2": _w((lambda *s: torch.randn(*s, dtype=torch.bfloat16) * 0.05)(F, D)),
            "b2": _w((lambda *s: torch.randn(*s, dtype=torch.bfloat16) * 0.05)(D)),
        }
    _B_in = _B_setup()

    return {
        "A_x": _A_in["x"],
        "A_wqkv": _A_in["wqkv"],
        "A_wo": _A_in["wo"],
        "B_x": _B_in["x"],
        "B_w1": _B_in["w1"],
        "B_b1": _B_in["b1"],
        "B_w2": _B_in["w2"],
        "B_b2": _B_in["b2"],
        "gate_w": _w((lambda *s: torch.randn(*s, dtype=torch.bfloat16) * 0.05)(640) * 16.0),
    }

def reference(A_x, A_wqkv, A_wo, B_x, B_w1, B_b1, B_w2, B_b2, gate_w):
    def _A_ref(x, wqkv, wo):
        B, S, D = x.shape
        H = 32; Dh = D // H
        qkv = (lambda x, *d: x.permute(*d))((x @ wqkv).reshape(B, S, 3, H, Dh), 2, 0, 3, 1, 4)
        q, k, v = qkv[0], qkv[1], qkv[2]
        phi = lambda t: torch.where(t > 0, t + 1, torch.exp(t)) + 1e-6
        pq, pk = phi(q), phi(k)
        kv = torch.einsum("bhsd,bhse->bhde", pk, v)
        z = torch.einsum("bhsd->bhd", pk)
        num = torch.einsum("bhqd,bhde->bhqe", pq, kv)
        den = torch.einsum("bhqd,bhd->bhq", pq, z)[..., None] + 1e-6
        o = num / den
        return (lambda x, *d: x.permute(*d))(o, 0, 2, 1, 3).reshape(B, S, D) @ wo

    def _B_ref(x, w1, b1, w2, b2):
        h = torch.einsum("bsd,df->bsf", x, w1) + b1
        zero = torch.zeros_like(h)
        r = torch.where(h > zero, h, zero)
        h = r * r
        out = torch.einsum("bsf,fd->bsd", h, w2) + b2
        return out

    x = A_x
    g = _sigmoid((x * gate_w).sum(-1, keepdims=True))
    a = _A_ref(x, A_wqkv, A_wo)
    b = _B_ref(x, B_w1, B_b1, B_w2, B_b2)
    return g * a + (1 - g) * b
```

```cpp
#include <hip/hip_runtime.h>
#include <hip/hip_bf16.h>
typedef unsigned short u16;
__device__ __forceinline__ float bf2f(u16 v){ return __uint_as_float(((unsigned)v)<<16); }
__device__ __forceinline__ u16 f2bf(float f){ __hip_bfloat16 b = __float2bfloat16(f); return *(u16*)&b; }
__device__ __forceinline__ float rbf(float f){ return bf2f(f2bf(f)); }

__global__ void gemm_naive(const u16* A, const u16* B, u16* C, int M, int N, int K){
  int n = blockIdx.x*blockDim.x + threadIdx.x; int m = blockIdx.y;
  if(n>=N) return;
  float acc=0.f;
  for(int k=0;k<K;++k) acc += bf2f(A[(long)m*K+k])*bf2f(B[(long)k*N+n]);
  C[(long)m*N+n]=f2bf(acc);
}
__device__ __forceinline__ float phi(float t){ float r = t>0.f ? rbf(t+1.f) : rbf(__expf(t)); return rbf(r+1e-6f); }
__global__ void kv_naive(const u16* qkv, u16* kv, u16* z){
  int h = blockIdx.x; int t = threadIdx.x;
  if(t<400){ int d=t/20,e=t%20; float acc=0.f;
    for(int s=0;s<2048;++s){ float pk=phi(bf2f(qkv[(long)s*1920+640+h*20+d])); acc+=pk*bf2f(qkv[(long)s*1920+1280+h*20+e]); }
    kv[h*400+t]=f2bf(acc);
  } else if(t<420){ int d=t-400; float acc=0.f;
    for(int s=0;s<2048;++s){ acc+=phi(bf2f(qkv[(long)s*1920+640+h*20+d])); }
    z[h*20+d]=f2bf(acc);
  }
}
__global__ void o_naive(const u16* qkv, const u16* kv, const u16* z, u16* o){
  int s = blockIdx.x; int c = threadIdx.x;
  int h=c/20,e=c%20; float num=0.f, den=0.f;
  for(int d=0;d<20;++d){ float pq=phi(bf2f(qkv[(long)s*1920+h*20+d])); num+=pq*bf2f(kv[h*400+d*20+e]); den+=pq*bf2f(z[h*20+d]); }
  num=rbf(num); den=rbf(rbf(den)+1e-6f);
  o[(long)s*640+c]=f2bf(num/den);
}
__global__ void act_naive(u16* h, const u16* b1){
  int s=blockIdx.x; for(int f=threadIdx.x; f<1536; f+=blockDim.x){ float v=rbf(bf2f(h[(long)s*1536+f])+bf2f(b1[f])); v = v>0.f? v:0.f; h[(long)s*1536+f]=f2bf(v*v);} }
__global__ void fin_naive(const u16* x, const u16* gw, const u16* a, const u16* b, const u16* b2, u16* out){
  int s=blockIdx.x; int c=threadIdx.x; __shared__ float red[640];
  red[c]=rbf(bf2f(x[(long)s*640+c])*bf2f(gw[c])); __syncthreads();
  __shared__ float gs;
  if(c==0){ float t=0.f; for(int i=0;i<640;++i) t+=red[i]; t=rbf(t); gs=rbf(1.f/(1.f+__expf(-t))); }
  __syncthreads();
  float g=gs; float av=bf2f(a[(long)s*640+c]); float bv=rbf(bf2f(b[(long)s*640+c])+bf2f(b2[c]));
  out[(long)s*640+c]=f2bf(rbf(g*av)+rbf(rbf(1.f-g)*bv));
}
extern "C" void kernel_launch(void* const* d_in, const int* in_sizes, int n_in,
                              void* d_out, int out_size, void* d_ws, size_t ws_size,
                              hipStream_t stream) {
  const u16* x=(const u16*)d_in[0]; const u16* wqkv=(const u16*)d_in[1]; const u16* wo=(const u16*)d_in[2];
  const u16* w1=(const u16*)d_in[4]; const u16* b1=(const u16*)d_in[5]; const u16* w2=(const u16*)d_in[6]; const u16* b2=(const u16*)d_in[7]; const u16* gw=(const u16*)d_in[8];
  u16* ws=(u16*)d_ws; u16* qkv=ws; u16* h=qkv+2048*1920; u16* kv=h+2048*1536; u16* z=kv+32*400; u16* o=z+640+64; u16* a=o+2048*640; u16* b=a+2048*640;
  gemm_naive<<<dim3(1920/128,2048),128,0,stream>>>(x,wqkv,qkv,2048,1920,640);
  gemm_naive<<<dim3(1536/128,2048),128,0,stream>>>(x,w1,h,2048,1536,640);
  kv_naive<<<32,448,0,stream>>>(qkv,kv,z);
  o_naive<<<2048,640,0,stream>>>(qkv,kv,z,o);
  act_naive<<<2048,256,0,stream>>>(h,b1);
  gemm_naive<<<dim3(640/128,2048),128,0,stream>>>(o,wo,a,2048,640,640);
  gemm_naive<<<dim3(640/128,2048),128,0,stream>>>(h,w2,b,2048,640,1536);
  fin_naive<<<2048,640,0,stream>>>(x,gw,a,b,b2,(u16*)d_out);
}
```

```cpp
#include <hip/hip_runtime.h>
#include <hip/hip_bf16.h>

typedef unsigned short u16;
typedef short bf16x8 __attribute__((ext_vector_type(8)));
typedef short s16x4 __attribute__((ext_vector_type(4)));
typedef float f32x4 __attribute__((ext_vector_type(4)));
typedef unsigned u32x4 __attribute__((ext_vector_type(4)));
typedef unsigned u32x2 __attribute__((ext_vector_type(2)));
typedef __attribute__((address_space(3))) s16x4* lds_s16x4_ptr;
#define DI __device__ __forceinline__
#define MFMA16(a, b, c) __builtin_amdgcn_mfma_f32_16x16x32_bf16((a), (b), (c), 0, 0, 0)

constexpr int SEQ = 2048, DM = 640, FF = 1536, NQKV = 1920;

constexpr size_t OFF_HF = 0;
constexpr size_t OFF_QF = OFF_HF + 128ull * 48 * 1024;
constexpr size_t OFF_AF = OFF_QF + 128ull * 20 * 1024;
constexpr size_t OFF_W2F = OFF_AF + 128ull * 20 * 1024;
constexpr size_t OFF_WPF = OFF_W2F + 48ull * 40 * 1024;
constexpr size_t OFF_KVP = OFF_WPF + 20ull * 40 * 1024;
constexpr size_t OFF_ZP = OFF_KVP + 16ull * 32 * 1600;
constexpr size_t OFF_G = OFF_ZP + 16ull * 640 * 4;
constexpr size_t OFF_FLAG = OFF_G + 2048 * 4;

DI unsigned pack2(float lo, float hi) {
  typedef __bf16 bf2 __attribute__((ext_vector_type(2)));
  typedef float f2 __attribute__((ext_vector_type(2)));
  f2 v = {lo, hi};
  bf2 r = __builtin_convertvector(v, bf2);
  return __builtin_bit_cast(unsigned, r);
}
DI float bf_lo(unsigned u) { return __uint_as_float(u << 16); }
DI float bf_hi(unsigned u) { return __uint_as_float(u & 0xffff0000u); }
DI float rbf(float f) { return bf_lo(pack2(f, 0.f) & 0xffffu); }
DI float phi(float t) {
  float e = __builtin_amdgcn_exp2f(t * 1.4426950408889634f);
  e = fminf(e, 1.0f);
  return fmaxf(t, 0.f) + e + 1e-6f;
}

constexpr int K1_STAGE = 45056;
constexpr int K1_LDS = 3 * K1_STAGE;

struct ColSrc { const u16* p; int stride; };
DI ColSrc other_src(int o, const u16* wqkv, const u16* w1) {
  ColSrc s;
  if (o < 40) { s.p = wqkv + 16 * o; s.stride = NQKV; }
  else { s.p = w1 + 16 * (o - 40); s.stride = FF; }
  return s;
}
template <bool KV> DI int slot_other(int nt, int j) {
  if (KV) return nt <= 6 ? 112 + 2 * j + (nt - 5) : 128 + j;
  else return 14 * j + (nt < 6 ? nt : (nt == 6 ? 12 : (nt == 13 ? 13 : nt - 1)));
}
template <bool KV> DI ColSrc slot_src(int nt, int j, const u16* wqkv, const u16* w1) {
  if (KV) {
    if (nt < 5) { ColSrc s; s.p = wqkv + 640 + 80 * j + 16 * nt; s.stride = NQKV; return s; }
    if (nt >= 7 && nt < 12) { ColSrc s; s.p = wqkv + 1280 + 80 * j + 16 * (nt - 7); s.stride = NQKV; return s; }
  }
  return other_src(slot_other<KV>(nt, j), wqkv, w1);
}

template <bool KV>
DI void k1_body(char* smem, int rb, int j, const u16* __restrict__ x, const u16* __restrict__ wqkv,
                const u16* __restrict__ w1, const u16* __restrict__ b1, char* __restrict__ ws) {
  const int tid = threadIdx.x;
  const int lane = tid & 63;
  const int w = __builtin_amdgcn_readfirstlane(tid >> 6);
  const int wm = w >> 1, wn = w & 1;
  const int g = lane >> 4, c = lane & 15;

  f32x4 acc[4][7];
  u32x2 bias[7];
#pragma unroll
  for (int ni = 0; ni < 7; ++ni) {
    bias[ni] = (u32x2){0u, 0u};
    if (!(KV && ni < 5)) {
      int o = slot_other<KV>(wn * 7 + ni, j);
      if (o >= 40) bias[ni] = *(const u32x2*)(b1 + 16 * (o - 40) + 4 * g);
    }
  }

  const u16* pa = x + (size_t)(rb * 128 + 32 * w + (lane >> 3)) * DM + (((lane & 7) ^ ((lane >> 3) & 7)) * 8);
  const u16* pb[7];
  int sb[7];
  {
    const int r = lane >> 2;
    const int cs = (lane & 3) ^ (((r >> 3) & 1) << 1);
#pragma unroll
    for (int p = 0; p < 7; ++p) {
      ColSrc s0 = slot_src<KV>(2 * p, j, wqkv, w1);
      ColSrc s1 = slot_src<KV>(2 * p + 1, j, wqkv, w1);
      const u16* bp = (cs >> 1) ? s1.p : s0.p;
      int st = (cs >> 1) ? s1.stride : s0.stride;
      pb[p] = bp + (size_t)(w * 16 + r) * st + (cs & 1) * 8;
      sb[p] = st * 64;
    }
  }
  auto stage = [&](int buf) {
    char* base = smem + buf * K1_STAGE;
#pragma unroll
    for (int i = 0; i < 4; ++i)
      __builtin_amdgcn_global_load_lds((const unsigned*)(pa + i * 8 * DM), (unsigned*)(base + (w * 4 + i) * 1024), 16, 0, 0);
#pragma unroll
    for (int p = 0; p < 7; ++p)
      __builtin_amdgcn_global_load_lds((const unsigned*)(pb[p]), (unsigned*)(base + 16384 + (w * 7 + p) * 1024), 16, 0, 0);
    pa += 64;
#pragma unroll
    for (int p = 0; p < 7; ++p) pb[p] += sb[p];
  };

  const int aoff0 = (wm * 64 + c) * 128 + ((g ^ (c & 7)) * 16);
  const int aoff1 = (wm * 64 + c) * 128 + (((g + 4) ^ (c & 7)) * 16);
  const int q4 = c >> 2, p4 = c & 3;
  const int bcommon = 16384 + (g >> 1) * 7168 + (8 * (g & 1) + q4) * 64 + (p4 & 1) * 8;
  const int sw = (g & 1) << 1;
  const int boffE = bcommon + (((2 * wn + (p4 >> 1)) ^ sw) * 16) + wn * 3 * 1024;
  const int boffO = bcommon + (((2 * (1 - wn) + (p4 >> 1)) ^ sw) * 16) + wn * 4 * 1024;

  bf16x8 fa0[4], fb0[7], fa1[4], fb1[7];
  auto rd = [&](bf16x8* fa, bf16x8* fb, int buf, int kk) {
    const char* base = smem + buf * K1_STAGE;
#pragma unroll
    for (int mi = 0; mi < 4; ++mi) fa[mi] = *(const bf16x8*)(base + (kk ? aoff1 : aoff0) + mi * 2048);
#pragma unroll
    for (int ni = 0; ni < 7; ++ni) {
      const char* bp = base + ((ni & 1) ? boffO : boffE) + (ni >> 1) * 1024 + kk * 14336;
      s16x4 lo = __builtin_amdgcn_ds_read_tr16_b64_v4i16((lds_s16x4_ptr)(bp));
      s16x4 hi = __builtin_amdgcn_ds_read_tr16_b64_v4i16((lds_s16x4_ptr)(bp + 256));
      fb[ni] = __builtin_shufflevector(lo, hi, 0, 1, 2, 3, 4, 5, 6, 7);
    }
  };
  auto mma = [&](bf16x8* fa, bf16x8* fb) {
#pragma unroll
    for (int ni = 0; ni < 7; ++ni)
#pragma unroll
      for (int mi = 0; mi < 4; ++mi) {
        if (KV && ni < 5) acc[mi][ni] = MFMA16(fa[mi], fb[ni], acc[mi][ni]);
        else acc[mi][ni] = MFMA16(fb[ni], fa[mi], acc[mi][ni]);
      }
  };

  asm volatile("" ::: "memory");
  stage(0);
  stage(1);
  asm volatile("s_waitcnt vmcnt(11)" ::: "memory");
  __builtin_amdgcn_s_barrier();
  asm volatile("" ::: "memory");
#pragma unroll
  for (int ni = 0; ni < 7; ++ni) {
    f32x4 init = {bf_lo(bias[ni][0]), bf_hi(bias[ni][0]), bf_lo(bias[ni][1]), bf_hi(bias[ni][1])};
#pragma unroll
    for (int mi = 0; mi < 4; ++mi) acc[mi][ni] = init;
  }
  asm volatile("" ::: "memory");
  stage(2);
  rd(fa0, fb0, 0, 0);
  rd(fa1, fb1, 0, 1);
  mma(fa0, fb0);
  int buf = 1, nbuf = 0;
  for (int s = 1; s < 10; ++s) {
    asm volatile("s_waitcnt lgkmcnt(0)" ::: "memory");
    if (s < 9) asm volatile("s_waitcnt vmcnt(11)" ::: "memory");
    else asm volatile("s_waitcnt vmcnt(0)" ::: "memory");
    __builtin_amdgcn_s_barrier();
    asm volatile("" ::: "memory");
    if (s < 8) stage(nbuf);
    rd(fa0, fb0, buf, 0);
    mma(fa1, fb1);
    rd(fa1, fb1, buf, 1);
    mma(fa0, fb0);
    nbuf = buf;
    buf = buf == 2 ? 0 : buf + 1;
  }
  mma(fa1, fb1);

  char* HF = ws + OFF_HF;
  char* QF = ws + OFF_QF;
  const int rt0 = rb * 8 + wm * 4;

  auto cvt_other = [&](const f32x4& a, int o) -> u32x2 {
    u32x2 r;
    if (o < 40) { r[0] = pack2(phi(a[0]), phi(a[1])); r[1] = pack2(phi(a[2]), phi(a[3])); }
    else {
      float t0 = fmaxf(a[0], 0.f), t1 = fmaxf(a[1], 0.f), t2 = fmaxf(a[2], 0.f), t3 = fmaxf(a[3], 0.f);
      r[0] = pack2(t0 * t0, t1 * t1); r[1] = pack2(t2 * t2, t3 * t3);
    }
    return r;
  };
  auto frag_ptr = [&](int o, int rt) -> char* {
    return o < 40 ? QF + ((size_t)rt * 20 + (o >> 1)) * 1024 : HF + ((size_t)rt * 48 + ((o - 40) >> 1)) * 1024;
  };
  auto store_pair = [&](int ni) {
    const int o = slot_other<KV>(wn * 7 + ni, j);
#pragma unroll
    for (int mi = 0; mi < 4; ++mi) {
      u32x2 lo = cvt_other(acc[mi][ni], o), hi = cvt_other(acc[mi][ni + 1], o);
      u32x4 v = {lo[0], lo[1], hi[0], hi[1]};
      *(u32x4*)(frag_ptr(o, rt0 + mi) + lane * 16) = v;
    }
  };
  auto store_single = [&](int ni) {
    const int o = slot_other<KV>(wn * 7 + ni, j);
#pragma unroll
    for (int mi = 0; mi < 4; ++mi) {
      u32x2 v = cvt_other(acc[mi][ni], o);
      *(u32x2*)(frag_ptr(o, rt0 + mi) + lane * 16 + (o & 1) * 8) = v;
    }
  };

  if (!KV) {
    store_pair(0); store_pair(2); store_pair(4); store_single(6);
  } else {
    asm volatile("s_waitcnt lgkmcnt(0)" ::: "memory");
    __builtin_amdgcn_s_barrier();
    asm volatile("" ::: "memory");
#pragma unroll
    for (int p = 0; p < 2; ++p)
#pragma unroll
      for (int t = 0; t < 5; ++t) {
        f32x4 a = acc[2 * p][t], b = acc[2 * p + 1][t];
        u32x4 v;
        if (wn == 0) {
          v[0] = pack2(phi(a[0]), phi(a[1])); v[1] = pack2(phi(a[2]), phi(a[3]));
          v[2] = pack2(phi(b[0]), phi(b[1])); v[3] = pack2(phi(b[2]), phi(b[3]));
        } else {
          v[0] = pack2(a[0], a[1]); v[1] = pack2(a[2], a[3]);
          v[2] = pack2(b[0], b[1]); v[3] = pack2(b[2], b[3]);
        }
        *(u32x4*)(smem + ((wn * 4 + wm * 2 + p) * 5 + t) * 1024 + lane * 16) = v;
      }
    if (wn == 0) store_pair(5); else store_single(5);
    __syncthreads();
    const unsigned tab = w == 0 ? 0x54440510u : 0u;
    (void)tab;
    float* KVP = (float*)(ws + OFF_KVP) + (size_t)rb * 32 * 400 + (size_t)j * 4 * 400;
    float* ZP = (float*)(ws + OFF_ZP) + rb * 640 + j * 80;
    const int nch = (w == 0 || w == 3) ? 5 : 4;
    for (int ch = 0; ch < nch; ++ch) {
      int te, td;
      if (w == 0) { te = ch == 0 ? 0 : ch == 1 ? 1 : ch == 2 ? 5 : ch == 3 ? 4 : 5; td = ch < 3 ? 0 : 4; }
      else if (ch < 4) { td = w; te = ch == 3 ? 5 : w - 1 + ch; }
      else { td = 4; te = 3; }
      f32x4 d = {0.f, 0.f, 0.f, 0.f};
#pragma unroll
      for (int ks = 0; ks < 4; ++ks) {
        bf16x8 pk = *(const bf16x8*)(smem + (ks * 5 + td) * 1024 + lane * 16);
        bf16x8 vv;
        if (te < 5) vv = *(const bf16x8*)(smem + ((4 + ks) * 5 + te) * 1024 + lane * 16);
        else vv = (bf16x8){0x3f80, 0x3f80, 0x3f80, 0x3f80, 0x3f80, 0x3f80, 0x3f80, 0x3f80};
        d = MFMA16(vv, pk, d);
      }
      const int dg = 16 * td + c;
      if (te < 5) {
        const int eg = 16 * te + 4 * g;
        const int hd = dg / 20, he = eg / 20;
        if (hd == he) *(f32x4*)(KVP + hd * 400 + (dg - hd * 20) * 20 + (eg - he * 20)) = d;
      } else if (g == 0) {
        ZP[dg] = d[0];
      }
    }
  }
}

__global__ __launch_bounds__(256, 1) void k1_kernel(const u16* __restrict__ x, const u16* __restrict__ wqkv,
                                                   const u16* __restrict__ w1, const u16* __restrict__ b1,
                                                   const u16* __restrict__ w2, char* __restrict__ ws) {
  extern __shared__ __attribute__((aligned(16))) char smem[];
  const int bid = blockIdx.x;
  const int xcd = bid & 7, idx = bid >> 3;
  const int rb = (xcd & 1) * 8 + (idx & 7);
  const int cq = (xcd >> 1) * 4 + (idx >> 3);

  const int lane = threadIdx.x & 63;
  const int gw = bid * 4 + (threadIdx.x >> 6);
  u16 wv[2][8];
  const int g = lane >> 4, c = lane & 15;
  if (gw < 960) {
#pragma unroll
    for (int i = 0; i < 2; ++i) {
      const int f = gw * 2 + i, kb = f / 40, nt = f % 40;
      const u16* src = w2 + (size_t)(kb * 32 + 4 * g) * DM + nt * 16 + c;
#pragma unroll
      for (int jj = 0; jj < 8; ++jj) wv[i][jj] = src[(size_t)((jj & 3) + (jj >> 2) * 16) * DM];
    }
  }

  if (cq & 1) k1_body<false>(smem, rb, cq >> 1, x, wqkv, w1, b1, ws);
  else k1_body<true>(smem, rb, cq >> 1, x, wqkv, w1, b1, ws);

  if (gw < 960) {
#pragma unroll
    for (int i = 0; i < 2; ++i) {
      u32x4 v;
#pragma unroll
      for (int jj = 0; jj < 4; ++jj) v[jj] = (unsigned)wv[i][2 * jj] | ((unsigned)wv[i][2 * jj + 1] << 16);
      *(u32x4*)(ws + OFF_W2F + (size_t)(gw * 2 + i) * 1024 + lane * 16) = v;
    }
  }
}

DI void task_w(int u, int lane, char* lds  , const u16* __restrict__ wo, char* ws) {
  const int h = u >> 3, cr = u & 7;
  const int g = lane >> 4, c = lane & 15;
  bf16x8 wf[5];
#pragma unroll
  for (int t = 0; t < 5; ++t)
#pragma unroll
    for (int jj = 0; jj < 8; ++jj) {
      const int e = 8 * g + jj;
      wf[t][jj] = e < 20 ? (short)wo[(size_t)(h * 20 + e) * DM + cr * 80 + 16 * t + c] : (short)0;
    }
  const float* KVP = (const float*)(ws + OFF_KVP) + h * 400;
  f32x4 s0 = {0.f, 0.f, 0.f, 0.f}, s1 = {0.f, 0.f, 0.f, 0.f};
#pragma unroll
  for (int p = 0; p < 16; ++p) {
    s0 += *(const f32x4*)(KVP + (size_t)p * 32 * 400 + lane * 4);
    if (lane < 36) s1 += *(const f32x4*)(KVP + (size_t)p * 32 * 400 + 256 + lane * 4);
  }
  *(u32x4*)(lds + lane * 32) = (u32x4){0u, 0u, 0u, 0u};
  *(u32x4*)(lds + lane * 32 + 16) = (u32x4){0u, 0u, 0u, 0u};
  __builtin_amdgcn_s_waitcnt(0xC07F);
  {
    int i0 = lane * 4, d0 = i0 / 20, e0 = i0 - d0 * 20;
    *(u32x2*)(lds + d0 * 64 + e0 * 2) = (u32x2){pack2(s0[0], s0[1]), pack2(s0[2], s0[3])};
    if (lane < 36) {
      int i1 = 256 + lane * 4, d1 = i1 / 20, e1 = i1 - d1 * 20;
      *(u32x2*)(lds + d1 * 64 + e1 * 2) = (u32x2){pack2(s1[0], s1[1]), pack2(s1[2], s1[3])};
    }
  }
  __builtin_amdgcn_s_waitcnt(0xC07F);
  bf16x8 ka0 = *(const bf16x8*)(lds + c * 64 + g * 16);
  bf16x8 ka1 = *(const bf16x8*)(lds + (16 + c) * 64 + g * 16);
  char* WPF = ws + OFF_WPF;
#pragma unroll
  for (int t = 0; t < 5; ++t) {
    f32x4 d0 = MFMA16(ka0, wf[t], ((f32x4){0.f, 0.f, 0.f, 0.f}));
    f32x4 d1 = MFMA16(ka1, wf[t], ((f32x4){0.f, 0.f, 0.f, 0.f}));
    {
      const int k = h * 20 + 4 * g;
      const int kb = k >> 5, half = (k >> 4) & 1, gg = (k & 15) >> 2;
      *(u32x2*)(WPF + ((size_t)kb * 40 + cr * 5 + t) * 1024 + (gg * 16 + c) * 16 + half * 8) =
          (u32x2){pack2(d0[0], d0[1]), pack2(d0[2], d0[3])};
    }
    if (g == 0) {
      const int k = h * 20 + 16;
      const int kb = k >> 5, half = (k >> 4) & 1, gg = (k & 15) >> 2;
      *(u32x2*)(WPF + ((size_t)kb * 40 + cr * 5 + t) * 1024 + (gg * 16 + c) * 16 + half * 8) =
          (u32x2){pack2(d1[0], d1[1]), pack2(d1[2], d1[3])};
    }
  }
}

DI void task_a(int u, int lane, char* lds  , const u16* __restrict__ x,
               const u16* __restrict__ gate_w, char* ws) {
  const int rt = u >> 1, kh = u & 1;
  const int g = lane >> 4, c = lane & 15;
  const float* ZP = (const float*)(ws + OFF_ZP) + kh * 320;
  f32x4 s0 = {0.f, 0.f, 0.f, 0.f}, s1 = {0.f, 0.f, 0.f, 0.f};
#pragma unroll
  for (int p = 0; p < 16; ++p) {
    s0 += *(const f32x4*)(ZP + p * 640 + lane * 4);
    if (lane < 16) s1 += *(const f32x4*)(ZP + p * 640 + 256 + lane * 4);
  }
  f32x4 tg = {0.f, 0.f, 0.f, 0.f};
#pragma unroll
  for (int kb = 0; kb < 20; ++kb) {
    bf16x8 xf = *(const bf16x8*)(x + (size_t)(rt * 16 + c) * DM + kb * 32 + 8 * g);
    bf16x8 gf = *(const bf16x8*)(gate_w + kb * 32 + 8 * g);
    tg = MFMA16(gf, xf, tg);
  }
  const float gate = 1.0f / (1.0f + __expf(-tg[0]));
  if (kh == 0 && g == 0) ((float*)(ws + OFF_G))[rt * 16 + c] = gate;

  *(u32x2*)(lds + lane * 8) = (u32x2){pack2(s0[0], s0[1]), pack2(s0[2], s0[3])};
  if (lane < 16) *(u32x2*)(lds + 512 + lane * 8) = (u32x2){pack2(s1[0], s1[1]), pack2(s1[2], s1[3])};
  __builtin_amdgcn_s_waitcnt(0xC07F);

  const char* QF = ws + OFF_QF + ((size_t)rt * 20 + kh * 10) * 1024;
  u32x4 qf[10];
#pragma unroll
  for (int b = 0; b < 10; ++b) qf[b] = *(const u32x4*)(QF + b * 1024 + lane * 16);
  f32x4 den = {0.f, 0.f, 0.f, 0.f};
#pragma unroll
  for (int b = 0; b < 10; ++b) {
    const int k0 = 32 * b + 4 * g, k1 = k0 + 16;
    u32x2 z0 = *(const u32x2*)(lds + k0 * 2), z1 = *(const u32x2*)(lds + k1 * 2);
    const bool m0 = (k0 / 20) == c, m1 = (k1 / 20) == c;
    u32x4 zf = {m0 ? z0[0] : 0u, m0 ? z0[1] : 0u, m1 ? z1[0] : 0u, m1 ? z1[1] : 0u};
    den = MFMA16(__builtin_bit_cast(bf16x8, zf), __builtin_bit_cast(bf16x8, qf[b]), den);
  }
  f32x4 rd;
#pragma unroll
  for (int r = 0; r < 4; ++r) rd[r] = gate / (den[r] + 1e-6f);
  *(f32x4*)(lds + 1024 + c * 64 + g * 16) = rd;
  __builtin_amdgcn_s_waitcnt(0xC07F);
  char* AF = ws + OFF_AF + ((size_t)rt * 20 + kh * 10) * 1024;
#pragma unroll
  for (int b = 0; b < 10; ++b) {
    const int k0 = 32 * b + 4 * g, k1 = k0 + 16;
    const float r0 = *(const float*)(lds + 1024 + c * 64 + (k0 / 20) * 4);
    const float r1 = *(const float*)(lds + 1024 + c * 64 + (k1 / 20) * 4);
    u32x4 o;
    o[0] = pack2(bf_lo(qf[b][0]) * r0, bf_hi(qf[b][0]) * r0);
    o[1] = pack2(bf_lo(qf[b][1]) * r0, bf_hi(qf[b][1]) * r0);
    o[2] = pack2(bf_lo(qf[b][2]) * r1, bf_hi(qf[b][2]) * r1);
    o[3] = pack2(bf_lo(qf[b][3]) * r1, bf_hi(qf[b][3]) * r1);
    *(u32x4*)(AF + b * 1024 + lane * 16) = o;
  }
}

__global__ __launch_bounds__(128) void kp_kernel(const u16* __restrict__ x, const u16* __restrict__ wo,
                                                 const u16* __restrict__ gate_w, char* ws) {
  __shared__ __attribute__((aligned(16))) char lds[4096];
  const int lane = threadIdx.x & 63;
  const int w = __builtin_amdgcn_readfirstlane(threadIdx.x >> 6);
  if (w == 0) task_w(blockIdx.x, lane, lds, wo, ws);
  else task_a(blockIdx.x, lane, lds + 2048, x, gate_w, ws);
}

constexpr int K2_DEPTH = 3;
__global__ __launch_bounds__(256, 1) void k2_kernel(const u16* __restrict__ b2, char* __restrict__ ws,
                                                   u16* __restrict__ out) {
  __shared__ __attribute__((aligned(16))) char red[4 * 20 * 1024];
  const int bid = blockIdx.x;
  const int xcd = bid & 7, idx = bid >> 3;
  const int rb = (xcd >> 1) * 8 + (idx & 7);
  const int cb = (xcd & 1) * 4 + (idx >> 3);
  const int lane = threadIdx.x & 63;
  const int w = __builtin_amdgcn_readfirstlane(threadIdx.x >> 6);
  const int g = lane >> 4, c = lane & 15;

  const char* HFp = ws + OFF_HF + ((size_t)rb * 4 * 48 + w) * 1024 + lane * 16;
  const char* AFp = ws + OFF_AF + ((size_t)rb * 4 * 20 + w) * 1024 + lane * 16;
  const char* W2p = ws + OFF_W2F + ((size_t)w * 40 + cb * 5) * 1024 + lane * 16;
  const char* WPp = ws + OFF_WPF + ((size_t)w * 40 + cb * 5) * 1024 + lane * 16;

  float gv[4];
#pragma unroll
  for (int mi = 0; mi < 4; ++mi) gv[mi] = 1.0f - ((const float*)(ws + OFF_G))[rb * 64 + mi * 16 + c];

  f32x4 acc[4][5];
#pragma unroll
  for (int mi = 0; mi < 4; ++mi)
#pragma unroll
    for (int ni = 0; ni < 5; ++ni) acc[mi][ni] = (f32x4){0.f, 0.f, 0.f, 0.f};

  bf16x8 fa[K2_DEPTH][4], fb[K2_DEPTH][5];
  auto load = [&](int i, int slot) {
    if (i < 12) {
#pragma unroll
      for (int mi = 0; mi < 4; ++mi) fa[slot][mi] = *(const bf16x8*)(HFp + (size_t)(mi * 48 + 4 * i) * 1024);
#pragma unroll
      for (int ni = 0; ni < 5; ++ni) fb[slot][ni] = *(const bf16x8*)(W2p + (size_t)(4 * i * 40 + ni) * 1024);
    } else {
      const int ii = i - 12;
#pragma unroll
      for (int mi = 0; mi < 4; ++mi) fa[slot][mi] = *(const bf16x8*)(AFp + (size_t)(mi * 20 + 4 * ii) * 1024);
#pragma unroll
      for (int ni = 0; ni < 5; ++ni) fb[slot][ni] = *(const bf16x8*)(WPp + (size_t)(4 * ii * 40 + ni) * 1024);
    }
  };
#pragma unroll
  for (int d = 0; d < K2_DEPTH; ++d) load(d, d);
#pragma unroll
  for (int i = 0; i < 17; ++i) {
    const int slot = i % K2_DEPTH;
    if (i == 12) {
#pragma unroll
      for (int mi = 0; mi < 4; ++mi)
#pragma unroll
        for (int ni = 0; ni < 5; ++ni) acc[mi][ni] *= gv[mi];
    }
#pragma unroll
    for (int mi = 0; mi < 4; ++mi)
#pragma unroll
      for (int ni = 0; ni < 5; ++ni) acc[mi][ni] = MFMA16(fb[slot][ni], fa[slot][mi], acc[mi][ni]);
    if (i + K2_DEPTH < 17) load(i + K2_DEPTH, slot);
  }

#pragma unroll
  for (int mi = 0; mi < 4; ++mi)
    if (mi != w) {
#pragma unroll
      for (int ni = 0; ni < 5; ++ni) *(f32x4*)(red + ((w * 4 + mi) * 5 + ni) * 1024 + lane * 16) = acc[mi][ni];
    }
  __syncthreads();
  float gw = gv[0];
  f32x4 mine[5];
#pragma unroll
  for (int ni = 0; ni < 5; ++ni) mine[ni] = acc[0][ni];
#pragma unroll
  for (int mi = 1; mi < 4; ++mi)
    if (w == mi) {
      gw = gv[mi];
#pragma unroll
      for (int ni = 0; ni < 5; ++ni) mine[ni] = acc[mi][ni];
    }
#pragma unroll
  for (int ni = 0; ni < 5; ++ni) {
    f32x4 s = mine[ni];
#pragma unroll
    for (int o = 1; o < 4; ++o) {
      const int ow = (w + o) & 3;
      s += *(const f32x4*)(red + ((ow * 4 + w) * 5 + ni) * 1024 + lane * 16);
    }
    const int n = cb * 80 + ni * 16 + 4 * g;
    u32x2 bv = *(const u32x2*)(b2 + n);
    s[0] += gw * bf_lo(bv[0]); s[1] += gw * bf_hi(bv[0]); s[2] += gw * bf_lo(bv[1]); s[3] += gw * bf_hi(bv[1]);
    *(u32x2*)(out + (size_t)(rb * 64 + w * 16 + c) * DM + n) = (u32x2){pack2(s[0], s[1]), pack2(s[2], s[3])};
  }
}

extern "C" void kernel_launch(void* const* d_in, const int* in_sizes, int n_in, void* d_out, int out_size,
                              void* d_ws, size_t ws_size, hipStream_t stream) {
  const u16* x = (const u16*)d_in[0];
  const u16* wqkv = (const u16*)d_in[1];
  const u16* wo = (const u16*)d_in[2];
  const u16* w1 = (const u16*)d_in[4];
  const u16* b1 = (const u16*)d_in[5];
  const u16* w2 = (const u16*)d_in[6];
  const u16* b2 = (const u16*)d_in[7];
  const u16* gw = (const u16*)d_in[8];
  char* ws = (char*)d_ws;
  (void)hipFuncSetAttribute((const void*)k1_kernel, hipFuncAttributeMaxDynamicSharedMemorySize, K1_LDS);
  k1_kernel<<<256, 256, K1_LDS, stream>>>(x, wqkv, w1, b1, w2, ws);
  kp_kernel<<<256, 128, 0, stream>>>(x, wo, gw, ws);
  k2_kernel<<<256, 256, 0, stream>>>(b2, ws, (u16*)d_out);
}
```

```cpp
#include <hip/hip_runtime.h>
#include <hip/hip_bf16.h>

typedef unsigned short u16;
typedef short bf16x8 __attribute__((ext_vector_type(8)));
typedef short s16x4 __attribute__((ext_vector_type(4)));
typedef float f32x4 __attribute__((ext_vector_type(4)));
typedef unsigned u32x4 __attribute__((ext_vector_type(4)));
typedef unsigned u32x2 __attribute__((ext_vector_type(2)));
typedef __attribute__((address_space(3))) s16x4* lds_s16x4_ptr;
#define DI __device__ __forceinline__
#define MFMA16(a, b, c) __builtin_amdgcn_mfma_f32_16x16x32_bf16((a), (b), (c), 0, 0, 0)

constexpr int SEQ = 2048, DM = 640, FF = 1536, NQKV = 1920;

constexpr size_t OFF_HF = 0;
constexpr size_t OFF_QF = OFF_HF + 128ull * 48 * 1024;
constexpr size_t OFF_AF = OFF_QF + 128ull * 20 * 1024;
constexpr size_t OFF_W2F = OFF_AF + 128ull * 20 * 1024;
constexpr size_t OFF_WPF = OFF_W2F + 48ull * 40 * 1024;
constexpr size_t OFF_KVP = OFF_WPF + 20ull * 40 * 1024;
constexpr size_t OFF_ZP = OFF_KVP + 16ull * 32 * 1600;
constexpr size_t OFF_G = OFF_ZP + 16ull * 640 * 4;
constexpr size_t OFF_FLAG = OFF_G + 2048 * 4;
constexpr size_t OFF_DBG = 64ull << 20;
#define STAMP(kern, idx)
#define RSTAMP(kern, idx)
#define LSTAMP(idx)

DI unsigned pack2(float lo, float hi) {
  typedef __bf16 bf2 __attribute__((ext_vector_type(2)));
  typedef float f2 __attribute__((ext_vector_type(2)));
  f2 v = {lo, hi};
  bf2 r = __builtin_convertvector(v, bf2);
  return __builtin_bit_cast(unsigned, r);
}
DI float bf_lo(unsigned u) { return __uint_as_float(u << 16); }
DI float bf_hi(unsigned u) { return __uint_as_float(u & 0xffff0000u); }
DI float rbf(float f) { return bf_lo(pack2(f, 0.f) & 0xffffu); }
DI float phi(float t) {
  float e = __builtin_amdgcn_exp2f(t * 1.4426950408889634f);
  e = fminf(e, 1.0f);
  return fmaxf(t, 0.f) + e + 1e-6f;
}

constexpr int K1_STAGE = 45056;
constexpr int K1_LDS = 3 * K1_STAGE;

struct ColSrc { const u16* p; int stride; };
template <bool KV> DI ColSrc pos_src(int pos, int j, const u16* wqkv, const u16* w1) {
  ColSrc s;
  if (KV) {
    if (pos < 5) { s.p = wqkv + 640 + 80 * j + 16 * pos; s.stride = NQKV; }
    else if (pos == 5) { s.p = wqkv + 16 * (32 + j); s.stride = NQKV; }
    else if (pos < 11) { s.p = wqkv + 1280 + 80 * j + 16 * (pos - 6); s.stride = NQKV; }
    else if (pos == 11) { s.p = wqkv + 1280 + 80 * j + 64; s.stride = NQKV; }
    else { s.p = w1 + 32 * (40 + j) + 16 * (pos - 12); s.stride = FF; }
  } else {
    if (pos < 2) { s.p = wqkv + 32 * (2 * j) + 16 * pos; s.stride = NQKV; }
    else if (pos < 6) { s.p = w1 + 32 * (5 * j) + 16 * (pos - 2); s.stride = FF; }
    else if (pos < 8) { s.p = wqkv + 32 * (2 * j + 1) + 16 * (pos - 6); s.stride = NQKV; }
    else { s.p = w1 + 32 * (5 * j + 2) + 16 * (pos - 8); s.stride = FF; }
  }
  return s;
}
template <bool KV> DI int slot_pos(int wn, int ni) {
  if (KV) {
    const int p0 = ni < 3 ? ni : (ni < 5 ? ni + 3 : ni + 7);
    const int p1 = ni < 2 ? ni + 3 : (ni < 5 ? ni + 6 : (ni == 5 ? 5 : 11));
    return wn ? p1 : p0;
  }
  return ni < 6 ? 6 * wn + ni : 12 + wn;
}
DI float relu_f(float v) { return __int_as_float(max(__float_as_int(v), 0)); }
DI float phi1(float t1) {
  float e = __builtin_amdgcn_exp2f(__builtin_fmaf(t1, 1.4426950408889634f, -1.4426950408889634e-6f));
  e = __builtin_amdgcn_fmed3f(e, 0.0f, 1.0f);
  return __int_as_float(max(__float_as_int(t1), __float_as_int(1e-6f))) + e;
}
DI u32x2 cvt_q(const f32x4& a) { return (u32x2){pack2(phi1(a[0]), phi1(a[1])), pack2(phi1(a[2]), phi1(a[3]))}; }
DI u32x2 cvt_h(const f32x4& a) {
  float t0 = relu_f(a[0]), t1 = relu_f(a[1]), t2 = relu_f(a[2]), t3 = relu_f(a[3]);
  return (u32x2){pack2(t0 * t0, t1 * t1), pack2(t2 * t2, t3 * t3)};
}
DI u32x2 cvt_v(const f32x4& a) { return (u32x2){pack2(a[0], a[1]), pack2(a[2], a[3])}; }

template <bool KV>
DI void k1_body(char* smem, int rb, int j, int ci, int ri, const u16* __restrict__ x, const u16* __restrict__ wqkv,
                const u16* __restrict__ w1, const u16* __restrict__ b1, const u16* __restrict__ w2,
                char* __restrict__ ws) {
  const int tid = threadIdx.x;
  const int lane = tid & 63;
  const int w = __builtin_amdgcn_readfirstlane(tid >> 6);
  const int wm = w >> 1, wn = w & 1;
  const int g = lane >> 4, c = lane & 15;
  RSTAMP(0, 30); STAMP(0, 0);

  const __amdgpu_buffer_rsrc_t rsX = __builtin_amdgcn_make_buffer_rsrc((void*)x, 0, SEQ * DM * 2, 0x00020000);
  const __amdgpu_buffer_rsrc_t rsQ = __builtin_amdgcn_make_buffer_rsrc((void*)wqkv, 0, DM * NQKV * 2, 0x00020000);
  const __amdgpu_buffer_rsrc_t rsW = __builtin_amdgcn_make_buffer_rsrc((void*)w1, 0, DM * FF * 2, 0x00020000);
  const int vA = (lane >> 3) * (DM * 2) + (((lane & 7) ^ ((lane >> 3) & 7)) * 16);
#define ROTB 1
#define ROTA2 5
  int ks = (ri * ROTB + (ci * ROTA2) / 2) % 10;
  int sA = (rb * 128 + 32 * w) * (DM * 2) + ks * 128;
  const int r16 = lane >> 2;
  const int cs = (lane & 3) ^ (((r16 >> 3) & 1) << 1);
  const int vQ = r16 * (NQKV * 2) + cs * 16;
  const int vW = r16 * (FF * 2) + cs * 16;
  const int vS = r16 * (NQKV * 2) + (cs & 1) * 16 + ((cs >> 1) ? 0 : ((640 + 80 * j + 64) - 16 * (32 + j)) * 2);
  const int vU = r16 * (NQKV * 2) + (cs & 1) * 16;
  int sQ = (w * 16 + ks * 64) * (NQKV * 2), sW = (w * 16 + ks * 64) * (FF * 2);
  auto st1 = [&](int buf, int i) {
    char* base = smem + buf * K1_STAGE;
    typedef __attribute__((address_space(3))) void* ldsp;
    if (i < 4) {
      __builtin_amdgcn_raw_ptr_buffer_load_lds(rsX, (ldsp)(base + (w * 4 + i) * 1024), 16, vA, sA + i * 8 * DM * 2, 0, 0);

    } else {
      const int p = i - 4;
      ldsp dst = (ldsp)(base + 16384 + (w * 7 + p) * 1024);
      const int col = (int)(pos_src<KV>((KV && p == 2) ? 5 : 2 * p, j, (const u16*)0, (const u16*)0).p - (const u16*)0) * 2;
      const bool isW = KV ? (p == 6) : !(p == 0 || p == 3);
      if (isW) __builtin_amdgcn_raw_ptr_buffer_load_lds(rsW, dst, 16, vW, sW + col, 0, 0);
      else __builtin_amdgcn_raw_ptr_buffer_load_lds(rsQ, dst, 16, (KV && p == 2) ? vS : ((KV && p == 5) ? vU : vQ), sQ + col, 0, 0);
      if (p == 6) {
        const int adv = ks == 9 ? -9 : 1;
        ks += adv; sA += adv * 128; sQ += adv * (64 * NQKV * 2); sW += adv * (64 * FF * 2);
      }
    }
  };
  asm volatile("" ::: "memory");
#pragma unroll
  for (int i = 0; i < 11; ++i) st1(0, i);
#pragma unroll
  for (int i = 0; i < 11; ++i) st1(1, i);
  asm volatile("" ::: "memory");

  {
    const u16* bsrc = b1 + (KV ? 32 * (40 + j) : 160 * j) + min(lane, KV ? 3 : 19) * 8;
    __builtin_amdgcn_global_load_lds((const unsigned*)bsrc, (unsigned*)(smem + K1_LDS + 1024 + w * 1024), 16, 0, 0);
  }
  asm volatile("" ::: "memory");

  const int aoff0 = (wm * 64 + c) * 128 + ((g ^ (c & 7)) * 16);
  const int aoff1 = (wm * 64 + c) * 128 + (((g + 4) ^ (c & 7)) * 16);
  const int q4 = c >> 2, p4 = c & 3;
  int boff[7];
#pragma unroll
  for (int ni = 0; ni < 7; ++ni) {
    const int pos = slot_pos<KV>(wn, ni);
    boff[ni] = 16384 + (g >> 1) * 7168 + (8 * (g & 1) + q4) * 64 + (p4 & 1) * 8 +
               (((2 * (pos & 1) + (p4 >> 1)) ^ ((g & 1) << 1)) * 16) + (pos >> 1) * 1024;
  }

  f32x4 acc[4][7];
  struct Frags { bf16x8 a[4]; s16x4 bl[7], bh[7]; };
  Frags F0, F1;
  auto rd1 = [&](Frags& F, int buf, int kk, int idx) {
    const char* base = smem + buf * K1_STAGE;
    if (idx < 4) F.a[idx] = *(const bf16x8*)(base + (kk ? aoff1 : aoff0) + idx * 2048);
    else {
      const int ni = (idx - 4) >> 1, hi = (idx - 4) & 1;
      const char* bp = base + boff[ni] + kk * 14336 + hi * 256;
      s16x4 v = __builtin_amdgcn_ds_read_tr16_b64_v4i16((lds_s16x4_ptr)(bp));
      if (hi) F.bh[ni] = v; else F.bl[ni] = v;
    }
  };
  auto rdst = [&](Frags& F, int buf, int kk, int nb, int p0, int np) {
#pragma unroll
    for (int i = 0; i < 6; ++i) {
      rd1(F, buf, kk, 3 * i); rd1(F, buf, kk, 3 * i + 1); rd1(F, buf, kk, 3 * i + 2);
      if (i < np) st1(nb, p0 + i);
    }
  };
  auto mma = [&](Frags& F) {
#pragma unroll
    for (int ni = 0; ni < 7; ++ni) {
      bf16x8 fb = __builtin_shufflevector(F.bl[ni], F.bh[ni], 0, 1, 2, 3, 4, 5, 6, 7);
#pragma unroll
      for (int mi = 0; mi < 4; ++mi) {
        if (KV && ni < 5) acc[mi][ni] = MFMA16(F.a[mi], fb, acc[mi][ni]);
        else acc[mi][ni] = MFMA16(fb, F.a[mi], acc[mi][ni]);
      }
    }
  };

#define K1_INTERLEAVE(NV)                                                   \
  _Pragma("unroll") for (int i_ = 0; i_ < 6; ++i_) {                        \
    __builtin_amdgcn_sched_group_barrier(0x008, 1, 0);                      \
    __builtin_amdgcn_sched_group_barrier(0x100, 1, 0);                      \
    __builtin_amdgcn_sched_group_barrier(0x008, 1, 0);                      \
    __builtin_amdgcn_sched_group_barrier(0x100, 1, 0);                      \
    __builtin_amdgcn_sched_group_barrier(0x008, 1, 0);                      \
    __builtin_amdgcn_sched_group_barrier(0x100, 1, 0);                      \
    if (i_ < (NV)) __builtin_amdgcn_sched_group_barrier(0x010, 1, 0);       \
    __builtin_amdgcn_sched_group_barrier(0x008, 1, 0);                      \
  }                                                                         \
  __builtin_amdgcn_sched_group_barrier(0x008, 4, 0);

  constexpr int NEXTRA = 1;
  asm volatile("s_waitcnt vmcnt(%0)" :: "n"(11 + NEXTRA) : "memory");
  __builtin_amdgcn_s_barrier();
  asm volatile("" ::: "memory");
  STAMP(0, 1);
#pragma unroll
  for (int ni = 0; ni < 7; ++ni) {
    float iv;
    if (KV) iv = ni < 2 ? 1e-6f : (ni == 2 ? (wn ? 0.f : 1e-6f) : (ni == 5 ? (wn ? 1e-6f : 0.f) : 0.f));
    else iv = ni < 2 ? 1e-6f : 0.f;
#pragma unroll
    for (int mi = 0; mi < 4; ++mi) acc[mi][ni] = (f32x4){iv, iv, iv, iv};
  }
  rdst(F0, 0, 0, 2, 0, 6);
  __builtin_amdgcn_sched_barrier(0);
  rdst(F1, 0, 1, 2, 6, 5);
  mma(F0);
  K1_INTERLEAVE(5)
  __builtin_amdgcn_sched_barrier(0);
  int buf = 1, nbuf = 0;
#pragma unroll 1
  for (int s = 1; s < 8; ++s) {
    asm volatile("s_waitcnt lgkmcnt(0)\n\ts_waitcnt vmcnt(11)" ::: "memory");
    LSTAMP(2 * s);
    __builtin_amdgcn_s_barrier();
    asm volatile("" ::: "memory");
    LSTAMP(2 * s + 1);
    __builtin_amdgcn_sched_barrier(0);
    rdst(F0, buf, 0, nbuf, 0, 6);
    mma(F1);
    K1_INTERLEAVE(6)
    __builtin_amdgcn_sched_barrier(0);
    rdst(F1, buf, 1, nbuf, 6, 5);
    mma(F0);
    K1_INTERLEAVE(5)
    __builtin_amdgcn_sched_barrier(0);
    nbuf = buf;
    buf = buf == 2 ? 0 : buf + 1;
  }
  STAMP(0, 2);
  asm volatile("s_waitcnt lgkmcnt(0)\n\ts_waitcnt vmcnt(11)" ::: "memory");
  __builtin_amdgcn_s_barrier();
  asm volatile("" ::: "memory");
  __builtin_amdgcn_sched_barrier(0);
  {
    const int gwv = min(blockIdx.x * 4 + w, 959);
#pragma unroll
    for (int i = 0; i < 2; ++i) {
      const int f = gwv * 2 + i, kb = f / 40, nt = f % 40;
      __builtin_amdgcn_global_load_lds((const unsigned*)(w2 + (size_t)(kb * 32 + (lane >> 1)) * DM + nt * 16 + (lane & 1) * 8),
                                       (unsigned*)(smem + K1_STAGE + (w * 2 + i) * 1024), 16, 0, 0);
    }
  }
  __builtin_amdgcn_sched_barrier(0);
  rdst(F0, 2, 0, 0, 0, 0);
  mma(F1);
  K1_INTERLEAVE(0)
  __builtin_amdgcn_sched_barrier(0);
  rdst(F1, 2, 1, 0, 0, 0);
  mma(F0);
  K1_INTERLEAVE(0)
  __builtin_amdgcn_sched_barrier(0);
  asm volatile("s_waitcnt lgkmcnt(0)\n\ts_waitcnt vmcnt(2)" ::: "memory");
  __builtin_amdgcn_s_barrier();
  asm volatile("" ::: "memory");
  __builtin_amdgcn_sched_barrier(0);
  rdst(F0, 0, 0, 0, 0, 0);
  mma(F1);
  K1_INTERLEAVE(0)
  __builtin_amdgcn_sched_barrier(0);
  rdst(F1, 0, 1, 0, 0, 0);
  mma(F0);
  K1_INTERLEAVE(0)
  __builtin_amdgcn_sched_barrier(0);
  mma(F1);
  STAMP(0, 3);

  asm volatile("s_waitcnt vmcnt(0)" ::: "memory");
  if (blockIdx.x * 4 + w < 960) {
#pragma unroll
    for (int i = 0; i < 2; ++i) {
      const char* bp = smem + K1_STAGE + (w * 2 + i) * 1024 + (4 * g + q4) * 32 + p4 * 8;
      s16x4 lo = __builtin_amdgcn_ds_read_tr16_b64_v4i16((lds_s16x4_ptr)(bp));
      s16x4 hi = __builtin_amdgcn_ds_read_tr16_b64_v4i16((lds_s16x4_ptr)(bp + 512));
      *(bf16x8*)(ws + OFF_W2F + (size_t)((blockIdx.x * 4 + w) * 2 + i) * 1024 + lane * 16) =
          __builtin_shufflevector(lo, hi, 0, 1, 2, 3, 4, 5, 6, 7);
    }
  }

  STAMP(0, 6);
  const int rt0 = rb * 8 + wm * 4;
  char* HF = ws + OFF_HF + (size_t)rt0 * 48 * 1024 + lane * 16;
  char* QF = ws + OFF_QF + (size_t)rt0 * 20 * 1024 + lane * 16;
  u32x2 bias[7];
  {
    const char* bl = smem + K1_LDS + 1024 + w * 1024 + g * 8;
#pragma unroll
    for (int ni = 0; ni < 7; ++ni) {
      if (KV) { if (ni >= 5) bias[ni] = *(const u32x2*)(bl + 32 * (ni - 5)); }
      else if (ni >= 2) bias[ni] = *(const u32x2*)(bl + (ni < 6 ? 128 * wn + 32 * (ni - 2) : 256 + 32 * wn));
    }
  }
  auto addb = [&](const f32x4& a, const u32x2& b) -> f32x4 {
    return a + (f32x4){bf_lo(b[0]), bf_hi(b[0]), bf_lo(b[1]), bf_hi(b[1])};
  };

  if (!KV) {
#pragma unroll
    for (int mi = 0; mi < 4; ++mi) {
      u32x2 lo = cvt_q(acc[mi][0]), hi = cvt_q(acc[mi][1]);
      *(u32x4*)(QF + (size_t)(mi * 20 + 2 * j + wn) * 1024) = (u32x4){lo[0], lo[1], hi[0], hi[1]};
    }
    STAMP(0, 7);
#pragma unroll
    for (int pr = 0; pr < 2; ++pr)
#pragma unroll
      for (int mi = 0; mi < 4; ++mi) {
        u32x2 lo = cvt_h(addb(acc[mi][2 + 2 * pr], bias[2 + 2 * pr])), hi = cvt_h(addb(acc[mi][3 + 2 * pr], bias[3 + 2 * pr]));
        *(u32x4*)(HF + (size_t)(mi * 48 + 5 * j + 2 * wn + pr) * 1024) = (u32x4){lo[0], lo[1], hi[0], hi[1]};
      }
    STAMP(0, 8); STAMP(0, 9);
#pragma unroll
    for (int mi = 0; mi < 4; ++mi)
      *(u32x2*)(HF + (size_t)(mi * 48 + 5 * j + 4) * 1024 + wn * 8) = cvt_h(addb(acc[mi][6], bias[6]));
  } else {
    asm volatile("s_waitcnt lgkmcnt(0)" ::: "memory");
    __builtin_amdgcn_s_barrier();
    asm volatile("" ::: "memory");
    STAMP(0, 7);
    char* xk = smem + (wm * 2) * 5 * 1024 + lane * 16;
    char* xv = xk + 20 * 1024;
    if (wn == 0) {
#pragma unroll
      for (int p = 0; p < 2; ++p) {
#pragma unroll
        for (int t = 0; t < 2; ++t) {
          u32x2 lo = cvt_v(acc[2 * p][3 + t]), hi = cvt_v(acc[2 * p + 1][3 + t]);
          *(u32x4*)(xv + (p * 5 + t) * 1024) = (u32x4){lo[0], lo[1], hi[0], hi[1]};
        }
#pragma unroll
        for (int t = 0; t < 3; ++t) {
          u32x2 lo = cvt_q(acc[2 * p][t]), hi = cvt_q(acc[2 * p + 1][t]);
          *(u32x4*)(xk + (p * 5 + t) * 1024) = (u32x4){lo[0], lo[1], hi[0], hi[1]};
        }
      }
    } else {
      *(u32x4*)(smem + 40 * 1024 + lane * 16) = (u32x4){0x3f803f80u, 0x3f803f80u, 0x3f803f80u, 0x3f803f80u};
#pragma unroll
      for (int p = 0; p < 2; ++p) {
#pragma unroll
        for (int t = 0; t < 3; ++t) {
          u32x2 lo = cvt_v(acc[2 * p][2 + t]), hi = cvt_v(acc[2 * p + 1][2 + t]);
          *(u32x4*)(xv + (p * 5 + 2 + t) * 1024) = (u32x4){lo[0], lo[1], hi[0], hi[1]};
        }
#pragma unroll
        for (int t = 0; t < 2; ++t) {
          u32x2 lo = cvt_q(acc[2 * p][t]), hi = cvt_q(acc[2 * p + 1][t]);
          *(u32x4*)(xk + (p * 5 + 3 + t) * 1024) = (u32x4){lo[0], lo[1], hi[0], hi[1]};
        }
      }
    }
    asm volatile("s_waitcnt lgkmcnt(0)" ::: "memory");
    __builtin_amdgcn_s_barrier();
    asm volatile("" ::: "memory");
    STAMP(0, 8);
    const unsigned TE = w == 0 ? 0x54510u : (w == 1 ? 0x55210u : (w == 2 ? 0x55321u : 0x35432u));
    const unsigned TD = w == 0 ? 0x44000u : (w == 1 ? 0x11111u : (w == 2 ? 0x22222u : 0x43333u));
    bf16x8 pk[5][4], vv[5][4];
#pragma unroll
    for (int ch = 0; ch < 5; ++ch) {
      const int te = (TE >> (4 * ch)) & 7, td = (TD >> (4 * ch)) & 7;
      const int vb = te < 5 ? (20 + te) * 1024 : 40 * 1024, vs = te < 5 ? 5 * 1024 : 0;
#pragma unroll
      for (int ks4 = 0; ks4 < 4; ++ks4) {
        pk[ch][ks4] = *(const bf16x8*)(smem + (ks4 * 5 + td) * 1024 + lane * 16);
        vv[ch][ks4] = *(const bf16x8*)(smem + vb + ks4 * vs + lane * 16);
      }
    }
    if (wn == 0) {
#pragma unroll
      for (int mi = 0; mi < 4; ++mi) {
        u32x2 lo = cvt_h(addb(acc[mi][5], bias[5])), hi = cvt_h(addb(acc[mi][6], bias[6]));
        *(u32x4*)(HF + (size_t)(mi * 48 + 40 + j) * 1024) = (u32x4){lo[0], lo[1], hi[0], hi[1]};
      }
    } else {
#pragma unroll
      for (int mi = 0; mi < 4; ++mi)
        *(u32x2*)(QF + (size_t)(mi * 20 + 16 + (j >> 1)) * 1024 + (j & 1) * 8) = cvt_q(acc[mi][5]);
    }
    STAMP(0, 9);
    float* KVP = (float*)(ws + OFF_KVP) + (size_t)rb * 32 * 400 + (size_t)j * 4 * 400;
    float* ZP = (float*)(ws + OFF_ZP) + rb * 640 + j * 80;
#pragma unroll
    for (int ch = 0; ch < 5; ++ch) {
      const int te = (TE >> (4 * ch)) & 7, td = (TD >> (4 * ch)) & 7;
      f32x4 d = {0.f, 0.f, 0.f, 0.f};
#pragma unroll
      for (int ks4 = 0; ks4 < 4; ++ks4) d = MFMA16(vv[ch][ks4], pk[ch][ks4], d);
      const int dg = 16 * td + c;
      if (te < 5) {
        const int eg = 16 * te + 4 * g;
        const int hd = dg / 20, he = eg / 20;
        if (hd == he) *(f32x4*)(KVP + hd * 400 + (dg - hd * 20) * 20 + (eg - he * 20)) = d;
      } else if (g == 0) {
        ZP[dg] = d[0];
      }
    }
  }
}

__global__ __launch_bounds__(256, 2) void k1_kernel(const u16* __restrict__ x, const u16* __restrict__ wqkv,
                                                   const u16* __restrict__ w1, const u16* __restrict__ b1,
                                                   const u16* __restrict__ w2, char* __restrict__ ws) {
  extern __shared__ __attribute__((aligned(16))) char smem[];
  const int bid = blockIdx.x;
  const int xcd = bid & 7, idx = bid >> 3;
  const int ri = idx & 7, ci = idx >> 3;
  const int rb = (xcd & 1) * 8 + ri;
  const int cq = (xcd >> 1) * 4 + ci;
  if (cq & 1) k1_body<false>(smem, rb, cq >> 1, ci, ri, x, wqkv, w1, b1, w2, ws);
  else k1_body<true>(smem, rb, cq >> 1, ci, ri, x, wqkv, w1, b1, w2, ws);
  STAMP(0, 4);
  STAMP(0, 5); RSTAMP(0, 31);
}

DI void task_w(int u, int lane, char* lds  , const u16* __restrict__ wo, char* ws) {
  const int h = u >> 3, cr = u & 7;
  const int g = lane >> 4, c = lane & 15;
  bf16x8 wf[5];
#pragma unroll
  for (int t = 0; t < 5; ++t)
#pragma unroll
    for (int jj = 0; jj < 8; ++jj) {
      const int e = 8 * g + jj;
      short vv = (short)wo[(size_t)(h * 20 + min(e, 19)) * DM + cr * 80 + 16 * t + c];
      wf[t][jj] = e < 20 ? vv : (short)0;
    }
  const float* KVP = (const float*)(ws + OFF_KVP) + h * 400;
  f32x4 s0 = {0.f, 0.f, 0.f, 0.f}, s1 = {0.f, 0.f, 0.f, 0.f};
#pragma unroll
  for (int p = 0; p < 16; ++p) {
    s0 += *(const f32x4*)(KVP + (size_t)p * 32 * 400 + lane * 4);
    s1 += *(const f32x4*)(KVP + (size_t)p * 32 * 400 + 256 + min(lane, 35) * 4);
  }
  *(u32x4*)(lds + lane * 32) = (u32x4){0u, 0u, 0u, 0u};
  *(u32x4*)(lds + lane * 32 + 16) = (u32x4){0u, 0u, 0u, 0u};
  __builtin_amdgcn_s_waitcnt(0xC07F);
  {
    int i0 = lane * 4, d0 = i0 / 20, e0 = i0 - d0 * 20;
    *(u32x2*)(lds + d0 * 64 + e0 * 2) = (u32x2){pack2(s0[0], s0[1]), pack2(s0[2], s0[3])};
    if (lane < 36) {
      int i1 = 256 + lane * 4, d1 = i1 / 20, e1 = i1 - d1 * 20;
      *(u32x2*)(lds + d1 * 64 + e1 * 2) = (u32x2){pack2(s1[0], s1[1]), pack2(s1[2], s1[3])};
    }
  }
  __builtin_amdgcn_s_waitcnt(0xC07F);
  bf16x8 ka0 = *(const bf16x8*)(lds + c * 64 + g * 16);
  bf16x8 ka1 = *(const bf16x8*)(lds + (16 + c) * 64 + g * 16);
  char* WPF = ws + OFF_WPF;
#pragma unroll
  for (int t = 0; t < 5; ++t) {
    f32x4 d0 = MFMA16(ka0, wf[t], ((f32x4){0.f, 0.f, 0.f, 0.f}));
    f32x4 d1 = MFMA16(ka1, wf[t], ((f32x4){0.f, 0.f, 0.f, 0.f}));
    {
      const int k = h * 20 + 4 * g;
      const int kb = k >> 5, half = (k >> 4) & 1, gg = (k & 15) >> 2;
      *(u32x2*)(WPF + ((size_t)kb * 40 + cr * 5 + t) * 1024 + (gg * 16 + c) * 16 + half * 8) =
          (u32x2){pack2(d0[0], d0[1]), pack2(d0[2], d0[3])};
    }
    if (g == 0) {
      const int k = h * 20 + 16;
      const int kb = k >> 5, half = (k >> 4) & 1, gg = (k & 15) >> 2;
      *(u32x2*)(WPF + ((size_t)kb * 40 + cr * 5 + t) * 1024 + (gg * 16 + c) * 16 + half * 8) =
          (u32x2){pack2(d1[0], d1[1]), pack2(d1[2], d1[3])};
    }
  }
}

DI void task_a(int u, int lane, char* lds  , const u16* __restrict__ x,
               const u16* __restrict__ gate_w, char* ws) {
  const int rt = u >> 1, kh = u & 1;
  const int g = lane >> 4, c = lane & 15;
  const float* ZP = (const float*)(ws + OFF_ZP) + kh * 320;
  f32x4 s0 = {0.f, 0.f, 0.f, 0.f}, s1 = {0.f, 0.f, 0.f, 0.f};
#pragma unroll
  for (int p = 0; p < 16; ++p) {
    s0 += *(const f32x4*)(ZP + p * 640 + lane * 4);
    s1 += *(const f32x4*)(ZP + p * 640 + 256 + (lane & 15) * 4);
  }
  f32x4 tg = {0.f, 0.f, 0.f, 0.f};
#pragma unroll
  for (int kb = 0; kb < 20; ++kb) {
    bf16x8 xf = *(const bf16x8*)(x + (size_t)(rt * 16 + c) * DM + kb * 32 + 8 * g);
    bf16x8 gf = *(const bf16x8*)(gate_w + kb * 32 + 8 * g);
    tg = MFMA16(gf, xf, tg);
  }
  const float gate = 1.0f / (1.0f + __expf(-tg[0]));
  if (kh == 0 && g == 0) ((float*)(ws + OFF_G))[rt * 16 + c] = gate;

  *(u32x2*)(lds + lane * 8) = (u32x2){pack2(s0[0], s0[1]), pack2(s0[2], s0[3])};
  if (lane < 16) *(u32x2*)(lds + 512 + lane * 8) = (u32x2){pack2(s1[0], s1[1]), pack2(s1[2], s1[3])};
  __builtin_amdgcn_s_waitcnt(0xC07F);

  const char* QF = ws + OFF_QF + ((size_t)rt * 20 + kh * 10) * 1024;
  u32x4 qf[10];
#pragma unroll
  for (int b = 0; b < 10; ++b) qf[b] = *(const u32x4*)(QF + b * 1024 + lane * 16);
  f32x4 den = {0.f, 0.f, 0.f, 0.f};
#pragma unroll
  for (int b = 0; b < 10; ++b) {
    const int k0 = 32 * b + 4 * g, k1 = k0 + 16;
    u32x2 z0 = *(const u32x2*)(lds + k0 * 2), z1 = *(const u32x2*)(lds + k1 * 2);
    const bool m0 = (k0 / 20) == c, m1 = (k1 / 20) == c;
    u32x4 zf = {m0 ? z0[0] : 0u, m0 ? z0[1] : 0u, m1 ? z1[0] : 0u, m1 ? z1[1] : 0u};
    den = MFMA16(__builtin_bit_cast(bf16x8, zf), __builtin_bit_cast(bf16x8, qf[b]), den);
  }
  f32x4 rd;
#pragma unroll
  for (int r = 0; r < 4; ++r) rd[r] = gate / (den[r] + 1e-6f);
  *(f32x4*)(lds + 1024 + c * 64 + g * 16) = rd;
  __builtin_amdgcn_s_waitcnt(0xC07F);
  char* AF = ws + OFF_AF + ((size_t)rt * 20 + kh * 10) * 1024;
#pragma unroll
  for (int b = 0; b < 10; ++b) {
    const int k0 = 32 * b + 4 * g, k1 = k0 + 16;
    const float r0 = *(const float*)(lds + 1024 + c * 64 + (k0 / 20) * 4);
    const float r1 = *(const float*)(lds + 1024 + c * 64 + (k1 / 20) * 4);
    u32x4 o;
    o[0] = pack2(bf_lo(qf[b][0]) * r0, bf_hi(qf[b][0]) * r0);
    o[1] = pack2(bf_lo(qf[b][1]) * r0, bf_hi(qf[b][1]) * r0);
    o[2] = pack2(bf_lo(qf[b][2]) * r1, bf_hi(qf[b][2]) * r1);
    o[3] = pack2(bf_lo(qf[b][3]) * r1, bf_hi(qf[b][3]) * r1);
    *(u32x4*)(AF + b * 1024 + lane * 16) = o;
  }
}

__global__ __launch_bounds__(128) void kp_kernel(const u16* __restrict__ x, const u16* __restrict__ wo,
                                                 const u16* __restrict__ gate_w, char* ws) {
  __shared__ __attribute__((aligned(16))) char lds[4096];
  const int lane = threadIdx.x & 63;
  const int w = __builtin_amdgcn_readfirstlane(threadIdx.x >> 6);
  if (w == 0) task_w(blockIdx.x, lane, lds, wo, ws);
  else task_a(blockIdx.x, lane, lds + 2048, x, gate_w, ws);
}

constexpr int K2_DEPTH = 3;
__global__ __launch_bounds__(256, 1) void k2_kernel(const u16* __restrict__ b2, char* __restrict__ ws,
                                                   u16* __restrict__ out) {
  __shared__ __attribute__((aligned(16))) char red[4 * 20 * 1024];
  const int bid = blockIdx.x;
  const int xcd = bid & 7, idx = bid >> 3;
  const int rb = (xcd >> 1) * 8 + (idx & 7);
  const int cb = (xcd & 1) * 4 + (idx >> 3);
  const int lane = threadIdx.x & 63;
  const int w = __builtin_amdgcn_readfirstlane(threadIdx.x >> 6);
  const int g = lane >> 4, c = lane & 15;

  const __amdgpu_buffer_rsrc_t rs = __builtin_amdgcn_make_buffer_rsrc(ws, 0, (int)OFF_KVP, 0x00020000);
  const int voff = lane * 16;
  const int oH = (int)OFF_HF + (rb * 4 * 48 + w) * 1024;
  const int oA = (int)OFF_AF + (rb * 4 * 20 + w) * 1024;
  const int oW2 = (int)OFF_W2F + (w * 40 + cb * 5) * 1024;
  const int oWP = (int)OFF_WPF + (w * 40 + cb * 5) * 1024;

  RSTAMP(2, 30); STAMP(2, 0);
  float gv[4];
#pragma unroll
  for (int mi = 0; mi < 4; ++mi) gv[mi] = 1.0f - ((const float*)(ws + OFF_G))[rb * 64 + mi * 16 + c];

  f32x4 acc[4][5];
#pragma unroll
  for (int mi = 0; mi < 4; ++mi)
#pragma unroll
    for (int ni = 0; ni < 5; ++ni) acc[mi][ni] = (f32x4){0.f, 0.f, 0.f, 0.f};

  constexpr int NS = K2_DEPTH + 1;
  u32x4 fa[NS][4], fb[NS][5];
  auto load = [&](int i, int slot) {
    if (i < 12) {
#pragma unroll
      for (int ni = 0; ni < 5; ++ni) fb[slot][ni] = __builtin_amdgcn_raw_buffer_load_b128(rs, voff, oW2 + (4 * i * 40 + ni) * 1024, 0);
#pragma unroll
      for (int mi = 0; mi < 4; ++mi) fa[slot][mi] = __builtin_amdgcn_raw_buffer_load_b128(rs, voff, oH + (mi * 48 + 4 * i) * 1024, 0);
    } else {
      const int ii = i - 12;
#pragma unroll
      for (int ni = 0; ni < 5; ++ni) fb[slot][ni] = __builtin_amdgcn_raw_buffer_load_b128(rs, voff, oWP + (4 * ii * 40 + ni) * 1024, 0);
#pragma unroll
      for (int mi = 0; mi < 4; ++mi) fa[slot][mi] = __builtin_amdgcn_raw_buffer_load_b128(rs, voff, oA + (mi * 20 + 4 * ii) * 1024, 0);
    }
  };
#pragma unroll
  for (int d = 0; d < K2_DEPTH; ++d) load(d, d);
  __builtin_amdgcn_sched_barrier(0);
#pragma unroll
  for (int i = 0; i < 17; ++i) {
    const int slot = i % NS;
    if (i == 1) { STAMP(2, 1); }
    if (i == 12) {
      STAMP(2, 2);
#pragma unroll
      for (int mi = 0; mi < 4; ++mi)
#pragma unroll
        for (int ni = 0; ni < 5; ++ni) acc[mi][ni] *= gv[mi];
      __builtin_amdgcn_sched_barrier(0);
    }
    if (i + K2_DEPTH < 17) load(i + K2_DEPTH, (i + K2_DEPTH) % NS);
#pragma unroll
    for (int mi = 0; mi < 4; ++mi)
#pragma unroll
      for (int ni = 0; ni < 5; ++ni)
        acc[mi][ni] = MFMA16(__builtin_bit_cast(bf16x8, fb[slot][ni]), __builtin_bit_cast(bf16x8, fa[slot][mi]), acc[mi][ni]);
    if (i + K2_DEPTH < 17) {
#pragma unroll
      for (int k = 0; k < 9; ++k) {
        __builtin_amdgcn_sched_group_barrier(0x008, 2, 0);
        __builtin_amdgcn_sched_group_barrier(0x020, 1, 0);
      }
      __builtin_amdgcn_sched_group_barrier(0x008, 2, 0);
    }
    __builtin_amdgcn_sched_barrier(0);
  }

  STAMP(2, 3);
#pragma unroll
  for (int mi = 0; mi < 4; ++mi)
    if (mi != w) {
#pragma unroll
      for (int ni = 0; ni < 5; ++ni) *(f32x4*)(red + ((w * 4 + mi) * 5 + ni) * 1024 + lane * 16) = acc[mi][ni];
    }
  __syncthreads();
  float gw = gv[0];
  f32x4 mine[5];
#pragma unroll
  for (int ni = 0; ni < 5; ++ni) mine[ni] = acc[0][ni];
#pragma unroll
  for (int mi = 1; mi < 4; ++mi)
    if (w == mi) {
      gw = gv[mi];
#pragma unroll
      for (int ni = 0; ni < 5; ++ni) mine[ni] = acc[mi][ni];
    }
#pragma unroll
  for (int ni = 0; ni < 5; ++ni) {
    f32x4 s = mine[ni];
#pragma unroll
    for (int o = 1; o < 4; ++o) {
      const int ow = (w + o) & 3;
      s += *(const f32x4*)(red + ((ow * 4 + w) * 5 + ni) * 1024 + lane * 16);
    }
    const int n = cb * 80 + ni * 16 + 4 * g;
    u32x2 bv = *(const u32x2*)(b2 + n);
    s[0] += gw * bf_lo(bv[0]); s[1] += gw * bf_hi(bv[0]); s[2] += gw * bf_lo(bv[1]); s[3] += gw * bf_hi(bv[1]);
    *(u32x2*)(out + (size_t)(rb * 64 + w * 16 + c) * DM + n) = (u32x2){pack2(s[0], s[1]), pack2(s[2], s[3])};
  }
  STAMP(2, 4);
  STAMP(2, 5); RSTAMP(2, 31);
}

extern "C" void kernel_launch(void* const* d_in, const int* in_sizes, int n_in, void* d_out, int out_size,
                              void* d_ws, size_t ws_size, hipStream_t stream) {
  const u16* x = (const u16*)d_in[0];
  const u16* wqkv = (const u16*)d_in[1];
  const u16* wo = (const u16*)d_in[2];
  const u16* w1 = (const u16*)d_in[4];
  const u16* b1 = (const u16*)d_in[5];
  const u16* w2 = (const u16*)d_in[6];
  const u16* b2 = (const u16*)d_in[7];
  const u16* gw = (const u16*)d_in[8];
  char* ws = (char*)d_ws;
  (void)hipFuncSetAttribute((const void*)k1_kernel, hipFuncAttributeMaxDynamicSharedMemorySize, K1_LDS + 6144);
  k1_kernel<<<256, 256, K1_LDS + 6144, stream>>>(x, wqkv, w1, b1, w2, ws);
  kp_kernel<<<256, 128, 0, stream>>>(x, wo, gw, ws);
  k2_kernel<<<256, 256, 0, stream>>>(b2, ws, (u16*)d_out);
}
```

```cpp
#include <hip/hip_runtime.h>
#include <hip/hip_bf16.h>

typedef unsigned short u16;
typedef short bf16x8 __attribute__((ext_vector_type(8)));
typedef short s16x4 __attribute__((ext_vector_type(4)));
typedef float f32x4 __attribute__((ext_vector_type(4)));
typedef unsigned u32x4 __attribute__((ext_vector_type(4)));
typedef unsigned u32x2 __attribute__((ext_vector_type(2)));
typedef __attribute__((address_space(3))) s16x4* lds_s16x4_ptr;
#define DI __device__ __forceinline__
#define MFMA16(a, b, c) __builtin_amdgcn_mfma_f32_16x16x32_bf16((a), (b), (c), 0, 0, 0)

constexpr int SEQ = 2048, DM = 640, FF = 1536, NQKV = 1920;

constexpr size_t OFF_HF = 0;
constexpr size_t OFF_QF = OFF_HF + 128ull * 48 * 1024;
constexpr size_t OFF_AF = OFF_QF + 128ull * 20 * 1024;
constexpr size_t OFF_W2F = OFF_AF + 128ull * 20 * 1024;
constexpr size_t OFF_WPF = OFF_W2F + 48ull * 40 * 1024;
constexpr size_t OFF_KVP = OFF_WPF + 20ull * 40 * 1024;
constexpr size_t OFF_ZP = OFF_KVP + 16ull * 32 * 1600;
constexpr size_t OFF_G = OFF_ZP + 16ull * 640 * 4;
constexpr size_t OFF_FLAG = OFF_G + 2048 * 4;
constexpr size_t OFF_DBG = 64ull << 20;
#define STAMP(kern, idx)
#define RSTAMP(kern, idx)
#define LSTAMP(idx)

DI unsigned pack2(float lo, float hi) {
  typedef __bf16 bf2 __attribute__((ext_vector_type(2)));
  typedef float f2 __attribute__((ext_vector_type(2)));
  f2 v = {lo, hi};
  bf2 r = __builtin_convertvector(v, bf2);
  return __builtin_bit_cast(unsigned, r);
}
DI float bf_lo(unsigned u) { return __uint_as_float(u << 16); }
DI float bf_hi(unsigned u) { return __uint_as_float(u & 0xffff0000u); }
DI float rbf(float f) { return bf_lo(pack2(f, 0.f) & 0xffffu); }
DI float phi(float t) {
  float e = __builtin_amdgcn_exp2f(t * 1.4426950408889634f);
  e = fminf(e, 1.0f);
  return fmaxf(t, 0.f) + e + 1e-6f;
}

constexpr int K1_STAGE = 45056;
constexpr int K1_LDS = 3 * K1_STAGE;

struct ColSrc { const u16* p; int stride; };
template <bool KV> DI ColSrc pos_src(int pos, int j, const u16* wqkv, const u16* w1) {
  ColSrc s;
  if (KV) {
    if (pos < 5) { s.p = wqkv + 640 + 80 * j + 16 * pos; s.stride = NQKV; }
    else if (pos == 5) { s.p = wqkv + 16 * (32 + j); s.stride = NQKV; }
    else if (pos < 11) { s.p = wqkv + 1280 + 80 * j + 16 * (pos - 6); s.stride = NQKV; }
    else if (pos == 11) { s.p = wqkv + 1280 + 80 * j + 64; s.stride = NQKV; }
    else { s.p = w1 + 32 * (40 + j) + 16 * (pos - 12); s.stride = FF; }
  } else {
    if (pos < 2) { s.p = wqkv + 32 * (2 * j) + 16 * pos; s.stride = NQKV; }
    else if (pos < 6) { s.p = w1 + 32 * (5 * j) + 16 * (pos - 2); s.stride = FF; }
    else if (pos < 8) { s.p = wqkv + 32 * (2 * j + 1) + 16 * (pos - 6); s.stride = NQKV; }
    else { s.p = w1 + 32 * (5 * j + 2) + 16 * (pos - 8); s.stride = FF; }
  }
  return s;
}
template <bool KV> DI int slot_pos(int wn, int ni) {
  if (KV) {
    const int p0 = ni < 3 ? ni : (ni < 5 ? ni + 3 : ni + 7);
    const int p1 = ni < 2 ? ni + 3 : (ni < 5 ? ni + 6 : (ni == 5 ? 5 : 11));
    return wn ? p1 : p0;
  }
  return ni < 6 ? 6 * wn + ni : 12 + wn;
}
DI float relu_f(float v) { return __int_as_float(max(__float_as_int(v), 0)); }
DI float phi1(float t1) {
  float e = __builtin_amdgcn_exp2f(__builtin_fmaf(t1, 1.4426950408889634f, -1.4426950408889634e-6f));
  e = __builtin_amdgcn_fmed3f(e, 0.0f, 1.0f);
  return __int_as_float(max(__float_as_int(t1), __float_as_int(1e-6f))) + e;
}
DI u32x2 cvt_q(const f32x4& a) { return (u32x2){pack2(phi1(a[0]), phi1(a[1])), pack2(phi1(a[2]), phi1(a[3]))}; }
DI u32x2 cvt_h(const f32x4& a) {
  float t0 = relu_f(a[0]), t1 = relu_f(a[1]), t2 = relu_f(a[2]), t3 = relu_f(a[3]);
  return (u32x2){pack2(t0 * t0, t1 * t1), pack2(t2 * t2, t3 * t3)};
}
DI u32x2 cvt_v(const f32x4& a) { return (u32x2){pack2(a[0], a[1]), pack2(a[2], a[3])}; }

template <bool KV>
DI void k1_body(char* smem, int rb, int j, int ci, int ri, const u16* __restrict__ x, const u16* __restrict__ wqkv,
                const u16* __restrict__ w1, const u16* __restrict__ b1, const u16* __restrict__ w2,
                const u16* __restrict__ gate_w, char* __restrict__ ws) {
  const int tid = threadIdx.x;
  const int lane = tid & 63;
  const int w = __builtin_amdgcn_readfirstlane(tid >> 6);
  const int wm = w >> 1, wn = w & 1;
  const int g = lane >> 4, c = lane & 15;
  RSTAMP(0, 30); STAMP(0, 0);

  const __amdgpu_buffer_rsrc_t rsX = __builtin_amdgcn_make_buffer_rsrc((void*)x, 0, SEQ * DM * 2, 0x00020000);
  const __amdgpu_buffer_rsrc_t rsQ = __builtin_amdgcn_make_buffer_rsrc((void*)wqkv, 0, DM * NQKV * 2, 0x00020000);
  const __amdgpu_buffer_rsrc_t rsW = __builtin_amdgcn_make_buffer_rsrc((void*)w1, 0, DM * FF * 2, 0x00020000);
  const int vA = (lane >> 3) * (DM * 2) + (((lane & 7) ^ ((lane >> 3) & 7)) * 16);
#define ROTB 1
#define ROTA2 5
  int ks = (ri * ROTB + (ci * ROTA2) / 2) % 10;
  int sA = (rb * 128 + 32 * w) * (DM * 2) + ks * 128;
  const int r16 = lane >> 2;
  const int cs = (lane & 3) ^ (((r16 >> 3) & 1) << 1);
  const int vQ = r16 * (NQKV * 2) + cs * 16;
  const int vW = r16 * (FF * 2) + cs * 16;
  const int vS = r16 * (NQKV * 2) + (cs & 1) * 16 + ((cs >> 1) ? 0 : ((640 + 80 * j + 64) - 16 * (32 + j)) * 2);
  const int vU = r16 * (NQKV * 2) + (cs & 1) * 16;
  int sQ = (w * 16 + ks * 64) * (NQKV * 2), sW = (w * 16 + ks * 64) * (FF * 2);
  auto st1 = [&](int buf, int i) {
    char* base = smem + buf * K1_STAGE;
    typedef __attribute__((address_space(3))) void* ldsp;
    if (i < 4) {
      __builtin_amdgcn_raw_ptr_buffer_load_lds(rsX, (ldsp)(base + (w * 4 + i) * 1024), 16, vA, sA + i * 8 * DM * 2, 0, 0);

    } else {
      const int p = i - 4;
      ldsp dst = (ldsp)(base + 16384 + (w * 7 + p) * 1024);
      const int col = (int)(pos_src<KV>((KV && p == 2) ? 5 : 2 * p, j, (const u16*)0, (const u16*)0).p - (const u16*)0) * 2;
      const bool isW = KV ? (p == 6) : !(p == 0 || p == 3);
      if (isW) __builtin_amdgcn_raw_ptr_buffer_load_lds(rsW, dst, 16, vW, sW + col, 0, 0);
      else __builtin_amdgcn_raw_ptr_buffer_load_lds(rsQ, dst, 16, (KV && p == 2) ? vS : ((KV && p == 5) ? vU : vQ), sQ + col, 0, 0);
      if (p == 6) {
        const int adv = ks == 9 ? -9 : 1;
        ks += adv; sA += adv * 128; sQ += adv * (64 * NQKV * 2); sW += adv * (64 * FF * 2);
      }
    }
  };
  asm volatile("" ::: "memory");
#pragma unroll
  for (int i = 0; i < 11; ++i) st1(0, i);
#pragma unroll
  for (int i = 0; i < 11; ++i) st1(1, i);
  asm volatile("" ::: "memory");

  {
    const u16* bsrc = b1 + (KV ? 32 * (40 + j) : 160 * j) + min(lane, KV ? 3 : 19) * 8;
    __builtin_amdgcn_global_load_lds((const unsigned*)bsrc, (unsigned*)(smem + K1_LDS + 1024 + w * 1024), 16, 0, 0);
  }
  asm volatile("" ::: "memory");

  const int aoff0 = (wm * 64 + c) * 128 + ((g ^ (c & 7)) * 16);
  const int aoff1 = (wm * 64 + c) * 128 + (((g + 4) ^ (c & 7)) * 16);
  const int q4 = c >> 2, p4 = c & 3;
  int boff[7];
#pragma unroll
  for (int ni = 0; ni < 7; ++ni) {
    const int pos = slot_pos<KV>(wn, ni);
    boff[ni] = 16384 + (g >> 1) * 7168 + (8 * (g & 1) + q4) * 64 + (p4 & 1) * 8 +
               (((2 * (pos & 1) + (p4 >> 1)) ^ ((g & 1) << 1)) * 16) + (pos >> 1) * 1024;
  }

  f32x4 acc[4][7];
  struct Frags { bf16x8 a[4]; s16x4 bl[7], bh[7]; };
  Frags F0, F1;
  auto rd1 = [&](Frags& F, int buf, int kk, int idx) {
    const char* base = smem + buf * K1_STAGE;
    if (idx < 4) F.a[idx] = *(const bf16x8*)(base + (kk ? aoff1 : aoff0) + idx * 2048);
    else {
      const int ni = (idx - 4) >> 1, hi = (idx - 4) & 1;
      const char* bp = base + boff[ni] + kk * 14336 + hi * 256;
      s16x4 v = __builtin_amdgcn_ds_read_tr16_b64_v4i16((lds_s16x4_ptr)(bp));
      if (hi) F.bh[ni] = v; else F.bl[ni] = v;
    }
  };
  auto rdst = [&](Frags& F, int buf, int kk, int nb, int p0, int np) {
#pragma unroll
    for (int i = 0; i < 6; ++i) {
      rd1(F, buf, kk, 3 * i); rd1(F, buf, kk, 3 * i + 1); rd1(F, buf, kk, 3 * i + 2);
      if (i < np) st1(nb, p0 + i);
    }
  };
  auto mma = [&](Frags& F) {
#pragma unroll
    for (int ni = 0; ni < 7; ++ni) {
      bf16x8 fb = __builtin_shufflevector(F.bl[ni], F.bh[ni], 0, 1, 2, 3, 4, 5, 6, 7);
#pragma unroll
      for (int mi = 0; mi < 4; ++mi) {
        if (KV && ni < 5) acc[mi][ni] = MFMA16(F.a[mi], fb, acc[mi][ni]);
        else acc[mi][ni] = MFMA16(fb, F.a[mi], acc[mi][ni]);
      }
    }
  };

#define K1_INTERLEAVE(NV)                                                   \
  _Pragma("unroll") for (int i_ = 0; i_ < 6; ++i_) {                        \
    __builtin_amdgcn_sched_group_barrier(0x008, 1, 0);                      \
    __builtin_amdgcn_sched_group_barrier(0x100, 1, 0);                      \
    __builtin_amdgcn_sched_group_barrier(0x008, 1, 0);                      \
    __builtin_amdgcn_sched_group_barrier(0x100, 1, 0);                      \
    __builtin_amdgcn_sched_group_barrier(0x008, 1, 0);                      \
    __builtin_amdgcn_sched_group_barrier(0x100, 1, 0);                      \
    if (i_ < (NV)) __builtin_amdgcn_sched_group_barrier(0x010, 1, 0);       \
    __builtin_amdgcn_sched_group_barrier(0x008, 1, 0);                      \
  }                                                                         \
  __builtin_amdgcn_sched_group_barrier(0x008, 4, 0);

  constexpr int NEXTRA = 1;
  asm volatile("s_waitcnt vmcnt(%0)" :: "n"(11 + NEXTRA) : "memory");
  __builtin_amdgcn_s_barrier();
  asm volatile("" ::: "memory");
  STAMP(0, 1);
#pragma unroll
  for (int ni = 0; ni < 7; ++ni) {
    float iv;
    if (KV) iv = ni < 2 ? 1e-6f : (ni == 2 ? (wn ? 0.f : 1e-6f) : (ni == 5 ? (wn ? 1e-6f : 0.f) : 0.f));
    else iv = ni < 2 ? 1e-6f : 0.f;
#pragma unroll
    for (int mi = 0; mi < 4; ++mi) acc[mi][ni] = (f32x4){iv, iv, iv, iv};
  }
  rdst(F0, 0, 0, 2, 0, 6);
  __builtin_amdgcn_sched_barrier(0);
  rdst(F1, 0, 1, 2, 6, 5);
  mma(F0);
  K1_INTERLEAVE(5)
  __builtin_amdgcn_sched_barrier(0);
  int buf = 1, nbuf = 0;
#pragma unroll 1
  for (int s = 1; s < 8; ++s) {
    asm volatile("s_waitcnt lgkmcnt(0)\n\ts_waitcnt vmcnt(11)" ::: "memory");
    LSTAMP(2 * s);
    __builtin_amdgcn_s_barrier();
    asm volatile("" ::: "memory");
    LSTAMP(2 * s + 1);
    __builtin_amdgcn_sched_barrier(0);
    rdst(F0, buf, 0, nbuf, 0, 6);
    mma(F1);
    K1_INTERLEAVE(6)
    __builtin_amdgcn_sched_barrier(0);
    rdst(F1, buf, 1, nbuf, 6, 5);
    mma(F0);
    K1_INTERLEAVE(5)
    __builtin_amdgcn_sched_barrier(0);
    nbuf = buf;
    buf = buf == 2 ? 0 : buf + 1;
  }
  STAMP(0, 2);
  asm volatile("s_waitcnt lgkmcnt(0)\n\ts_waitcnt vmcnt(11)" ::: "memory");
  __builtin_amdgcn_s_barrier();
  asm volatile("" ::: "memory");
  __builtin_amdgcn_sched_barrier(0);
  {
    const int gwv = min(blockIdx.x * 4 + w, 959);
#pragma unroll
    for (int i = 0; i < 2; ++i) {
      const int f = gwv * 2 + i, kb = f / 40, nt = f % 40;
      __builtin_amdgcn_global_load_lds((const unsigned*)(w2 + (size_t)(kb * 32 + (lane >> 1)) * DM + nt * 16 + (lane & 1) * 8),
                                       (unsigned*)(smem + K1_STAGE + (w * 2 + i) * 1024), 16, 0, 0);
    }
  }
  __builtin_amdgcn_sched_barrier(0);
  rdst(F0, 2, 0, 0, 0, 0);
  mma(F1);
  K1_INTERLEAVE(0)
  __builtin_amdgcn_sched_barrier(0);
  rdst(F1, 2, 1, 0, 0, 0);
  mma(F0);
  K1_INTERLEAVE(0)
  __builtin_amdgcn_sched_barrier(0);
  asm volatile("s_waitcnt lgkmcnt(0)\n\ts_waitcnt vmcnt(2)" ::: "memory");
  __builtin_amdgcn_s_barrier();
  asm volatile("" ::: "memory");
  __builtin_amdgcn_sched_barrier(0);
  rdst(F0, 0, 0, 0, 0, 0);
  mma(F1);
  K1_INTERLEAVE(0)
  __builtin_amdgcn_sched_barrier(0);
  rdst(F1, 0, 1, 0, 0, 0);
  mma(F0);
  K1_INTERLEAVE(0)
  __builtin_amdgcn_sched_barrier(0);
  mma(F1);
  STAMP(0, 3);

  asm volatile("s_waitcnt vmcnt(0)" ::: "memory");
  if (blockIdx.x * 4 + w < 960) {
#pragma unroll
    for (int i = 0; i < 2; ++i) {
      const char* bp = smem + K1_STAGE + (w * 2 + i) * 1024 + (4 * g + q4) * 32 + p4 * 8;
      s16x4 lo = __builtin_amdgcn_ds_read_tr16_b64_v4i16((lds_s16x4_ptr)(bp));
      s16x4 hi = __builtin_amdgcn_ds_read_tr16_b64_v4i16((lds_s16x4_ptr)(bp + 512));
      *(bf16x8*)(ws + OFF_W2F + (size_t)((blockIdx.x * 4 + w) * 2 + i) * 1024 + lane * 16) =
          __builtin_shufflevector(lo, hi, 0, 1, 2, 3, 4, 5, 6, 7);
    }
  }

  STAMP(0, 6);
  const int rt0 = rb * 8 + wm * 4;
  char* HF = ws + OFF_HF + (size_t)rt0 * 48 * 1024 + lane * 16;
  char* QF = ws + OFF_QF + (size_t)rt0 * 20 * 1024 + lane * 16;
  u32x2 bias[7];
  {
    const char* bl = smem + K1_LDS + 1024 + w * 1024 + g * 8;
#pragma unroll
    for (int ni = 0; ni < 7; ++ni) {
      if (KV) { if (ni >= 5) bias[ni] = *(const u32x2*)(bl + 32 * (ni - 5)); }
      else if (ni >= 2) bias[ni] = *(const u32x2*)(bl + (ni < 6 ? 128 * wn + 32 * (ni - 2) : 256 + 32 * wn));
    }
  }
  auto addb = [&](const f32x4& a, const u32x2& b) -> f32x4 {
    return a + (f32x4){bf_lo(b[0]), bf_hi(b[0]), bf_lo(b[1]), bf_hi(b[1])};
  };

  if (!KV) {
    const int grow = rb * 128 + j * 16 + w * 4 + g;
    u32x4 gx[5], gwv[5];
#pragma unroll
    for (int i = 0; i < 5; ++i) {
      gx[i] = *(const u32x4*)(x + (size_t)grow * DM + (i * 16 + c) * 8);
      gwv[i] = *(const u32x4*)(gate_w + (i * 16 + c) * 8);
    }
#pragma unroll
    for (int mi = 0; mi < 4; ++mi) {
      u32x2 lo = cvt_q(acc[mi][0]), hi = cvt_q(acc[mi][1]);
      *(u32x4*)(QF + (size_t)(mi * 20 + 2 * j + wn) * 1024) = (u32x4){lo[0], lo[1], hi[0], hi[1]};
    }
    STAMP(0, 7);
#pragma unroll
    for (int pr = 0; pr < 2; ++pr)
#pragma unroll
      for (int mi = 0; mi < 4; ++mi) {
        u32x2 lo = cvt_h(addb(acc[mi][2 + 2 * pr], bias[2 + 2 * pr])), hi = cvt_h(addb(acc[mi][3 + 2 * pr], bias[3 + 2 * pr]));
        *(u32x4*)(HF + (size_t)(mi * 48 + 5 * j + 2 * wn + pr) * 1024) = (u32x4){lo[0], lo[1], hi[0], hi[1]};
      }
    STAMP(0, 8); STAMP(0, 9);
#pragma unroll
    for (int mi = 0; mi < 4; ++mi)
      *(u32x2*)(HF + (size_t)(mi * 48 + 5 * j + 4) * 1024 + wn * 8) = cvt_h(addb(acc[mi][6], bias[6]));
    {
      typedef __bf16 bf2 __attribute__((ext_vector_type(2)));
      float t0 = 0.f, t1 = 0.f;
#pragma unroll
      for (int i = 0; i < 5; ++i)
#pragma unroll
        for (int e = 0; e < 4; e += 2) {
          const unsigned xa = gx[i][e], wa = gwv[i][e], xb = gx[i][e + 1], wb = gwv[i][e + 1];
          t0 = __builtin_amdgcn_fdot2_f32_bf16(__builtin_bit_cast(bf2, xa), __builtin_bit_cast(bf2, wa), t0, false);
          t1 = __builtin_amdgcn_fdot2_f32_bf16(__builtin_bit_cast(bf2, xb), __builtin_bit_cast(bf2, wb), t1, false);
        }
      float t = t0 + t1;
      t += __shfl_xor(t, 1); t += __shfl_xor(t, 2); t += __shfl_xor(t, 4); t += __shfl_xor(t, 8);
      if (c == 0) ((float*)(ws + OFF_G))[grow] = 1.0f / (1.0f + __expf(-t));
    }
  } else {
    asm volatile("s_waitcnt lgkmcnt(0)" ::: "memory");
    __builtin_amdgcn_s_barrier();
    asm volatile("" ::: "memory");
    STAMP(0, 7);
    char* xk = smem + (wm * 2) * 5 * 1024 + lane * 16;
    char* xv = xk + 20 * 1024;
    if (wn == 0) {
#pragma unroll
      for (int p = 0; p < 2; ++p) {
#pragma unroll
        for (int t = 0; t < 2; ++t) {
          u32x2 lo = cvt_v(acc[2 * p][3 + t]), hi = cvt_v(acc[2 * p + 1][3 + t]);
          *(u32x4*)(xv + (p * 5 + t) * 1024) = (u32x4){lo[0], lo[1], hi[0], hi[1]};
        }
#pragma unroll
        for (int t = 0; t < 3; ++t) {
          u32x2 lo = cvt_q(acc[2 * p][t]), hi = cvt_q(acc[2 * p + 1][t]);
          *(u32x4*)(xk + (p * 5 + t) * 1024) = (u32x4){lo[0], lo[1], hi[0], hi[1]};
        }
      }
    } else {
      *(u32x4*)(smem + 40 * 1024 + lane * 16) = (u32x4){0x3f803f80u, 0x3f803f80u, 0x3f803f80u, 0x3f803f80u};
#pragma unroll
      for (int p = 0; p < 2; ++p) {
#pragma unroll
        for (int t = 0; t < 3; ++t) {
          u32x2 lo = cvt_v(acc[2 * p][2 + t]), hi = cvt_v(acc[2 * p + 1][2 + t]);
          *(u32x4*)(xv + (p * 5 + 2 + t) * 1024) = (u32x4){lo[0], lo[1], hi[0], hi[1]};
        }
#pragma unroll
        for (int t = 0; t < 2; ++t) {
          u32x2 lo = cvt_q(acc[2 * p][t]), hi = cvt_q(acc[2 * p + 1][t]);
          *(u32x4*)(xk + (p * 5 + 3 + t) * 1024) = (u32x4){lo[0], lo[1], hi[0], hi[1]};
        }
      }
    }
    asm volatile("s_waitcnt lgkmcnt(0)" ::: "memory");
    __builtin_amdgcn_s_barrier();
    asm volatile("" ::: "memory");
    STAMP(0, 8);
    const unsigned TE = w == 0 ? 0x54510u : (w == 1 ? 0x55210u : (w == 2 ? 0x55321u : 0x35432u));
    const unsigned TD = w == 0 ? 0x44000u : (w == 1 ? 0x11111u : (w == 2 ? 0x22222u : 0x43333u));
    bf16x8 pk[5][4], vv[5][4];
#pragma unroll
    for (int ch = 0; ch < 5; ++ch) {
      const int te = (TE >> (4 * ch)) & 7, td = (TD >> (4 * ch)) & 7;
      const int vb = te < 5 ? (20 + te) * 1024 : 40 * 1024, vs = te < 5 ? 5 * 1024 : 0;
#pragma unroll
      for (int ks4 = 0; ks4 < 4; ++ks4) {
        pk[ch][ks4] = *(const bf16x8*)(smem + (ks4 * 5 + td) * 1024 + lane * 16);
        vv[ch][ks4] = *(const bf16x8*)(smem + vb + ks4 * vs + lane * 16);
      }
    }
    if (wn == 0) {
#pragma unroll
      for (int mi = 0; mi < 4; ++mi) {
        u32x2 lo = cvt_h(addb(acc[mi][5], bias[5])), hi = cvt_h(addb(acc[mi][6], bias[6]));
        *(u32x4*)(HF + (size_t)(mi * 48 + 40 + j) * 1024) = (u32x4){lo[0], lo[1], hi[0], hi[1]};
      }
    } else {
#pragma unroll
      for (int mi = 0; mi < 4; ++mi)
        *(u32x2*)(QF + (size_t)(mi * 20 + 16 + (j >> 1)) * 1024 + (j & 1) * 8) = cvt_q(acc[mi][5]);
    }
    STAMP(0, 9);
    float* KVP = (float*)(ws + OFF_KVP) + (size_t)rb * 32 * 400 + (size_t)j * 4 * 400;
    float* ZP = (float*)(ws + OFF_ZP) + rb * 640 + j * 80;
#pragma unroll
    for (int ch = 0; ch < 5; ++ch) {
      const int te = (TE >> (4 * ch)) & 7, td = (TD >> (4 * ch)) & 7;
      f32x4 d = {0.f, 0.f, 0.f, 0.f};
#pragma unroll
      for (int ks4 = 0; ks4 < 4; ++ks4) d = MFMA16(vv[ch][ks4], pk[ch][ks4], d);
      const int dg = 16 * td + c;
      if (te < 5) {
        const int eg = 16 * te + 4 * g;
        const int hd = dg / 20, he = eg / 20;
        if (hd == he) *(f32x4*)(KVP + hd * 400 + (dg - hd * 20) * 20 + (eg - he * 20)) = d;
      } else if (g == 0) {
        ZP[dg] = d[0];
      }
    }
  }
}

__global__ __launch_bounds__(256, 2) void k1_kernel(const u16* __restrict__ x, const u16* __restrict__ wqkv,
                                                   const u16* __restrict__ w1, const u16* __restrict__ b1,
                                                   const u16* __restrict__ w2, const u16* __restrict__ gate_w,
                                                   char* __restrict__ ws) {
  extern __shared__ __attribute__((aligned(16))) char smem[];
  const int bid = blockIdx.x;
  const int xcd = bid & 7, idx = bid >> 3;
  const int ri = idx & 7, ci = idx >> 3;
  const int rb = (xcd & 1) * 8 + ri;
  const int cq = (xcd >> 1) * 4 + ci;
  if (bid == 0 && threadIdx.x < 64) ((unsigned*)(ws + OFF_FLAG))[threadIdx.x * 64] = 0u;
  if (cq & 1) k1_body<false>(smem, rb, cq >> 1, ci, ri, x, wqkv, w1, b1, w2, gate_w, ws);
  else k1_body<true>(smem, rb, cq >> 1, ci, ri, x, wqkv, w1, b1, w2, gate_w, ws);
  STAMP(0, 4);
  STAMP(0, 5); RSTAMP(0, 31);
}

typedef __attribute__((address_space(1))) unsigned gu32;
#define RLX_AGENT __ATOMIC_RELAXED, __HIP_MEMORY_SCOPE_AGENT
DI void publish(char* ws, int counter, int lane) {
  asm volatile("s_waitcnt vmcnt(0)" ::: "memory");
  if (lane == 0) __hip_atomic_fetch_add((gu32*)(ws + OFF_FLAG) + counter * 64, 1u, RLX_AGENT);
}
DI void task_w(int u, int lane, char* lds  , const u16* __restrict__ wo, char* ws) {
  const __amdgpu_buffer_rsrc_t rs = __builtin_amdgcn_make_buffer_rsrc(ws, 0, (int)OFF_FLAG, 0x00020000);
  const int h = u >> 3, cr = u & 7;
  const int g = lane >> 4, c = lane & 15;
  const float* KVP = (const float*)(ws + OFF_KVP) + h * 400;
  f32x4 k0[16], k1[16];
#pragma unroll
  for (int p = 0; p < 16; ++p) {
    k0[p] = *(const f32x4*)(KVP + (size_t)p * 32 * 400 + lane * 4);
    k1[p] = *(const f32x4*)(KVP + (size_t)p * 32 * 400 + 256 + min(lane, 35) * 4);
  }
#pragma unroll
  for (int i = 0; i < 4; ++i) {
    const int q = min(i * 64 + lane, 199), row = q / 10, ch = q - row * 10;
    __builtin_amdgcn_global_load_lds((const unsigned*)(wo + (size_t)(h * 20 + row) * DM + cr * 80 + ch * 8),
                                     (unsigned*)(lds + 2048 + i * 1024), 16, 0, 0);
  }
  __builtin_amdgcn_sched_barrier(0);
  __builtin_amdgcn_s_barrier();
  __builtin_amdgcn_sched_barrier(0);
  f32x4 s0 = k0[0], s1 = k1[0];
#pragma unroll
  for (int p = 1; p < 16; ++p) { s0 += k0[p]; s1 += k1[p]; }
  asm volatile("s_waitcnt vmcnt(0)" ::: "memory");
  bf16x8 wf[5];
  {
    const int r0 = min(8 * g + (c >> 2), 19), r1 = min(8 * g + 4 + (c >> 2), 19);
#pragma unroll
    for (int t = 0; t < 5; ++t) {
      s16x4 lo = __builtin_amdgcn_ds_read_tr16_b64_v4i16((lds_s16x4_ptr)(lds + 2048 + r0 * 160 + t * 32 + (c & 3) * 8));
      s16x4 hi = __builtin_amdgcn_ds_read_tr16_b64_v4i16((lds_s16x4_ptr)(lds + 2048 + r1 * 160 + t * 32 + (c & 3) * 8));
      wf[t] = __builtin_shufflevector(lo, hi, 0, 1, 2, 3, 4, 5, 6, 7);
    }
  }
  *(u32x4*)(lds + lane * 32) = (u32x4){0u, 0u, 0u, 0u};
  *(u32x4*)(lds + lane * 32 + 16) = (u32x4){0u, 0u, 0u, 0u};
  __builtin_amdgcn_s_waitcnt(0xC07F);
  {
    int i0 = lane * 4, d0 = i0 / 20, e0 = i0 - d0 * 20;
    *(u32x2*)(lds + d0 * 64 + e0 * 2) = (u32x2){pack2(s0[0], s0[1]), pack2(s0[2], s0[3])};
    if (lane < 36) {
      int i1 = 256 + lane * 4, d1 = i1 / 20, e1 = i1 - d1 * 20;
      *(u32x2*)(lds + d1 * 64 + e1 * 2) = (u32x2){pack2(s1[0], s1[1]), pack2(s1[2], s1[3])};
    }
  }
  __builtin_amdgcn_s_waitcnt(0xC07F);
  bf16x8 ka0 = *(const bf16x8*)(lds + c * 64 + g * 16);
  bf16x8 ka1 = *(const bf16x8*)(lds + (16 + c) * 64 + g * 16);
#pragma unroll
  for (int t = 0; t < 5; ++t) {
    f32x4 d0 = MFMA16(ka0, wf[t], ((f32x4){0.f, 0.f, 0.f, 0.f}));
    f32x4 d1 = MFMA16(ka1, wf[t], ((f32x4){0.f, 0.f, 0.f, 0.f}));
    {
      const int k = h * 20 + 4 * g;
      const int kb = k >> 5, half = (k >> 4) & 1, gg = (k & 15) >> 2;
      __builtin_amdgcn_raw_buffer_store_b64((u32x2){pack2(d0[0], d0[1]), pack2(d0[2], d0[3])}, rs,
          (int)OFF_WPF + (kb * 40 + cr * 5 + t) * 1024 + half * 512 + (gg * 16 + c) * 8, 0, 16);
    }
    if (g == 0) {
      const int k = h * 20 + 16;
      const int kb = k >> 5, half = (k >> 4) & 1, gg = (k & 15) >> 2;
      __builtin_amdgcn_raw_buffer_store_b64((u32x2){pack2(d1[0], d1[1]), pack2(d1[2], d1[3])}, rs,
          (int)OFF_WPF + (kb * 40 + cr * 5 + t) * 1024 + half * 512 + (gg * 16 + c) * 8, 0, 16);
    }
  }
  publish(ws, 32 + cr, lane);
}

DI void task_a(int u, int lane, char* lds  , char* ws) {
  const __amdgpu_buffer_rsrc_t rs = __builtin_amdgcn_make_buffer_rsrc(ws, 0, (int)OFF_FLAG, 0x00020000);
  const int rt = u >> 1, kh = u & 1;
  const int g = lane >> 4, c = lane & 15;
  const float* ZP = (const float*)(ws + OFF_ZP) + kh * 320;
  f32x4 z0[16], z1[16];
#pragma unroll
  for (int p = 0; p < 16; ++p) {
    z0[p] = *(const f32x4*)(ZP + p * 640 + lane * 4);
    z1[p] = *(const f32x4*)(ZP + p * 640 + 256 + (lane & 15) * 4);
  }
  const char* QF = ws + OFF_QF + ((size_t)rt * 20 + kh * 10) * 1024;
  u32x4 qf[10];
#pragma unroll
  for (int b = 0; b < 10; ++b) qf[b] = *(const u32x4*)(QF + b * 1024 + lane * 16);
  const float gate = ((const float*)(ws + OFF_G))[rt * 16 + c];
  __builtin_amdgcn_sched_barrier(0);
  __builtin_amdgcn_s_barrier();
  __builtin_amdgcn_sched_barrier(0);
  f32x4 s0 = z0[0], s1 = z1[0];
#pragma unroll
  for (int p = 1; p < 16; ++p) { s0 += z0[p]; s1 += z1[p]; }
  *(u32x2*)(lds + lane * 8) = (u32x2){pack2(s0[0], s0[1]), pack2(s0[2], s0[3])};
  if (lane < 16) *(u32x2*)(lds + 512 + lane * 8) = (u32x2){pack2(s1[0], s1[1]), pack2(s1[2], s1[3])};
  __builtin_amdgcn_s_waitcnt(0xC07F);
  f32x4 den = {0.f, 0.f, 0.f, 0.f};
#pragma unroll
  for (int b = 0; b < 10; ++b) {
    const int k0 = 32 * b + 4 * g, k1 = k0 + 16;
    u32x2 zl = *(const u32x2*)(lds + k0 * 2), zh = *(const u32x2*)(lds + k1 * 2);
    const bool m0 = (k0 / 20) == c, m1 = (k1 / 20) == c;
    u32x4 zf = {m0 ? zl[0] : 0u, m0 ? zl[1] : 0u, m1 ? zh[0] : 0u, m1 ? zh[1] : 0u};
    den = MFMA16(__builtin_bit_cast(bf16x8, zf), __builtin_bit_cast(bf16x8, qf[b]), den);
  }
  f32x4 rd;
#pragma unroll
  for (int r = 0; r < 4; ++r) rd[r] = gate / (den[r] + 1e-6f);
  *(f32x4*)(lds + 1024 + c * 64 + g * 16) = rd;
  __builtin_amdgcn_s_waitcnt(0xC07F);
#pragma unroll
  for (int b = 0; b < 10; ++b) {
    const int k0 = 32 * b + 4 * g, k1 = k0 + 16;
    const float r0 = *(const float*)(lds + 1024 + c * 64 + (k0 / 20) * 4);
    const float r1 = *(const float*)(lds + 1024 + c * 64 + (k1 / 20) * 4);
    u32x4 o;
    o[0] = pack2(bf_lo(qf[b][0]) * r0, bf_hi(qf[b][0]) * r0);
    o[1] = pack2(bf_lo(qf[b][1]) * r0, bf_hi(qf[b][1]) * r0);
    o[2] = pack2(bf_lo(qf[b][2]) * r1, bf_hi(qf[b][2]) * r1);
    o[3] = pack2(bf_lo(qf[b][3]) * r1, bf_hi(qf[b][3]) * r1);
    __builtin_amdgcn_raw_buffer_store_b128(o, rs, (int)OFF_AF + (rt * 20 + kh * 10 + b) * 1024 + lane * 16, 0, 16);
  }
  publish(ws, rt >> 2, lane);
}

constexpr int K2_DEPTH = 3;
__global__ __launch_bounds__(384, 2) void k2_kernel(const u16* __restrict__ wo, const u16* __restrict__ b2,
                                                   char* ws, u16* __restrict__ out) {
  __shared__ __attribute__((aligned(16))) char red[4 * 20 * 1024 + 8192 + 16];
  typedef __attribute__((address_space(3))) unsigned lu32;
  lu32* ready = (lu32*)(red + 81920 + 8192);
  const int bid = blockIdx.x;
  const int xcd = bid & 7, idx = bid >> 3;
  const int rb = (xcd >> 1) * 8 + (idx & 7);
  const int cb = (xcd & 1) * 4 + (idx >> 3);
  const int lane = threadIdx.x & 63;
  const int w = __builtin_amdgcn_readfirstlane(threadIdx.x >> 6);
  const int g = lane >> 4, c = lane & 15;

  if (w >= 4) {
    __builtin_amdgcn_s_setprio(3);
    RSTAMP(2, 30); STAMP(2, 0);
    if (w == 4) {
      if (lane == 0) __hip_atomic_store(ready, 0u, __ATOMIC_RELAXED, __HIP_MEMORY_SCOPE_WORKGROUP);
      asm volatile("s_waitcnt lgkmcnt(0)" ::: "memory");
      task_w(bid, lane, red + 81920, wo, ws);
      STAMP(2, 1);
    } else {
      task_a(bid, lane, red + 81920 + 6144, ws);
      STAMP(2, 1);
      gu32* cntA = (gu32*)(ws + OFF_FLAG) + rb * 64;
      gu32* cntW = (gu32*)(ws + OFF_FLAG) + (32 + cb) * 64;
      for (unsigned spins = 0; spins < (1u << 22); ++spins) {
        const unsigned a = __hip_atomic_load(cntA, RLX_AGENT), b = __hip_atomic_load(cntW, RLX_AGENT);
        if (__builtin_amdgcn_readfirstlane(a) == 8u && __builtin_amdgcn_readfirstlane(b) == 32u) break;
        __builtin_amdgcn_s_sleep(4);
      }
      if (lane == 0) __hip_atomic_store(ready, 1u, __ATOMIC_RELAXED, __HIP_MEMORY_SCOPE_WORKGROUP);
    }
    STAMP(2, 2); STAMP(2, 3); STAMP(2, 4); STAMP(2, 5); RSTAMP(2, 31);
    return;
  }

  const __amdgpu_buffer_rsrc_t rs = __builtin_amdgcn_make_buffer_rsrc(ws, 0, (int)OFF_FLAG, 0x00020000);
  const int voff = lane * 16;
  const int oH = (int)OFF_HF + (rb * 4 * 48 + w) * 1024;
  const int oA = (int)OFF_AF + (rb * 4 * 20 + w) * 1024;
  const int oW2 = (int)OFF_W2F + (w * 40 + cb * 5) * 1024;
  const int oWP = (int)OFF_WPF + (w * 40 + cb * 5) * 1024;

  RSTAMP(2, 30); STAMP(2, 0);
  f32x4 acc[4][5];
#pragma unroll
  for (int mi = 0; mi < 4; ++mi)
#pragma unroll
    for (int ni = 0; ni < 5; ++ni) acc[mi][ni] = (f32x4){0.f, 0.f, 0.f, 0.f};

  constexpr int NS = K2_DEPTH + 1;
  u32x4 fa[NS][4], fb[NS][5];
  float gv[4];
  auto load = [&](int i, int slot) {
    if (i < 12) {
#pragma unroll
      for (int ni = 0; ni < 5; ++ni) fb[slot][ni] = __builtin_amdgcn_raw_buffer_load_b128(rs, voff, oW2 + (4 * i * 40 + ni) * 1024, 0);
#pragma unroll
      for (int mi = 0; mi < 4; ++mi) fa[slot][mi] = __builtin_amdgcn_raw_buffer_load_b128(rs, voff, oH + (mi * 48 + 4 * i) * 1024, 0);
    } else {
      const int ii = i - 12;
#pragma unroll
      for (int ni = 0; ni < 5; ++ni) {
        u32x2 lo = __builtin_amdgcn_raw_buffer_load_b64(rs, lane * 8, oWP + (4 * ii * 40 + ni) * 1024, 16);
        u32x2 hi = __builtin_amdgcn_raw_buffer_load_b64(rs, lane * 8, oWP + (4 * ii * 40 + ni) * 1024 + 512, 16);
        fb[slot][ni] = (u32x4){lo[0], lo[1], hi[0], hi[1]};
      }
#pragma unroll
      for (int mi = 0; mi < 4; ++mi) fa[slot][mi] = __builtin_amdgcn_raw_buffer_load_b128(rs, voff, oA + (mi * 20 + 4 * ii) * 1024, 16);
    }
  };
  __builtin_amdgcn_s_barrier();
  __builtin_amdgcn_sched_barrier(0);
#pragma unroll
  for (int d = 0; d < K2_DEPTH; ++d) load(d, d);
#pragma unroll
  for (int mi = 0; mi < 4; ++mi)
    gv[mi] = __uint_as_float(__builtin_amdgcn_raw_buffer_load_b32(rs, c * 4, (int)OFF_G + (rb * 64 + mi * 16) * 4, 0));
  __builtin_amdgcn_sched_barrier(0);
#pragma unroll
  for (int i = 0; i < 17; ++i) {
    const int slot = i % NS;
    if (i == 1) { STAMP(2, 1); }
    if (i == 12 - K2_DEPTH) {
      STAMP(2, 6);
      for (unsigned spins = 0; spins < (1u << 24); ++spins) {
        if (__builtin_amdgcn_readfirstlane(__hip_atomic_load(ready, __ATOMIC_RELAXED, __HIP_MEMORY_SCOPE_WORKGROUP)) != 0u) break;
        __builtin_amdgcn_s_sleep(1);
      }
      asm volatile("" ::: "memory");
      STAMP(2, 7);
      __builtin_amdgcn_sched_barrier(0);
    }
    if (i == 12) {
      STAMP(2, 2);
#pragma unroll
      for (int mi = 0; mi < 4; ++mi) {
        gv[mi] = 1.0f - gv[mi];
#pragma unroll
        for (int ni = 0; ni < 5; ++ni) acc[mi][ni] *= gv[mi];
      }
      __builtin_amdgcn_sched_barrier(0);
    }
    if (i + K2_DEPTH < 17) load(i + K2_DEPTH, (i + K2_DEPTH) % NS);
#pragma unroll
    for (int mi = 0; mi < 4; ++mi)
#pragma unroll
      for (int ni = 0; ni < 5; ++ni)
        acc[mi][ni] = MFMA16(__builtin_bit_cast(bf16x8, fb[slot][ni]), __builtin_bit_cast(bf16x8, fa[slot][mi]), acc[mi][ni]);
    if (i + K2_DEPTH < 12) {
#pragma unroll
      for (int k = 0; k < 9; ++k) {
        __builtin_amdgcn_sched_group_barrier(0x008, 2, 0);
        __builtin_amdgcn_sched_group_barrier(0x020, 1, 0);
      }
      __builtin_amdgcn_sched_group_barrier(0x008, 2, 0);
    } else if (i + K2_DEPTH < 17) {
#pragma unroll
      for (int k = 0; k < 14; ++k) {
        __builtin_amdgcn_sched_group_barrier(0x008, 1, 0);
        __builtin_amdgcn_sched_group_barrier(0x020, 1, 0);
      }
      __builtin_amdgcn_sched_group_barrier(0x008, 6, 0);
    }
    __builtin_amdgcn_sched_barrier(0);
  }

  STAMP(2, 3);
#pragma unroll
  for (int mi = 0; mi < 4; ++mi)
    if (mi != w) {
#pragma unroll
      for (int ni = 0; ni < 5; ++ni) *(f32x4*)(red + ((w * 4 + mi) * 5 + ni) * 1024 + lane * 16) = acc[mi][ni];
    }
  __syncthreads();
  float gw = gv[0];
  f32x4 mine[5];
#pragma unroll
  for (int ni = 0; ni < 5; ++ni) mine[ni] = acc[0][ni];
#pragma unroll
  for (int mi = 1; mi < 4; ++mi)
    if (w == mi) {
      gw = gv[mi];
#pragma unroll
      for (int ni = 0; ni < 5; ++ni) mine[ni] = acc[mi][ni];
    }
#pragma unroll
  for (int ni = 0; ni < 5; ++ni) {
    f32x4 s = mine[ni];
#pragma unroll
    for (int o = 1; o < 4; ++o) {
      const int ow = (w + o) & 3;
      s += *(const f32x4*)(red + ((ow * 4 + w) * 5 + ni) * 1024 + lane * 16);
    }
    const int n = cb * 80 + ni * 16 + 4 * g;
    u32x2 bv = *(const u32x2*)(b2 + n);
    s[0] += gw * bf_lo(bv[0]); s[1] += gw * bf_hi(bv[0]); s[2] += gw * bf_lo(bv[1]); s[3] += gw * bf_hi(bv[1]);
    *(u32x2*)(out + (size_t)(rb * 64 + w * 16 + c) * DM + n) = (u32x2){pack2(s[0], s[1]), pack2(s[2], s[3])};
  }
  STAMP(2, 4);
  STAMP(2, 5); RSTAMP(2, 31);
}

extern "C" void kernel_launch(void* const* d_in, const int* in_sizes, int n_in, void* d_out, int out_size,
                              void* d_ws, size_t ws_size, hipStream_t stream) {
  const u16* x = (const u16*)d_in[0];
  const u16* wqkv = (const u16*)d_in[1];
  const u16* wo = (const u16*)d_in[2];
  const u16* w1 = (const u16*)d_in[4];
  const u16* b1 = (const u16*)d_in[5];
  const u16* w2 = (const u16*)d_in[6];
  const u16* b2 = (const u16*)d_in[7];
  const u16* gw = (const u16*)d_in[8];
  char* ws = (char*)d_ws;
  (void)hipFuncSetAttribute((const void*)k1_kernel, hipFuncAttributeMaxDynamicSharedMemorySize, K1_LDS + 6144);
  k1_kernel<<<256, 256, K1_LDS + 6144, stream>>>(x, wqkv, w1, b1, w2, gw, ws);
  k2_kernel<<<256, 384, 0, stream>>>(wo, b2, ws, (u16*)d_out);
}
```

```cpp
#include <hip/hip_runtime.h>
#include <hip/hip_bf16.h>

typedef unsigned short u16;
typedef short bf16x8 __attribute__((ext_vector_type(8)));
typedef short s16x4 __attribute__((ext_vector_type(4)));
typedef float f32x4 __attribute__((ext_vector_type(4)));
typedef unsigned u32x4 __attribute__((ext_vector_type(4)));
typedef unsigned u32x2 __attribute__((ext_vector_type(2)));
typedef __attribute__((address_space(3))) s16x4* lds_s16x4_ptr;
#define DI __device__ __forceinline__
#define MFMA16(a, b, c) __builtin_amdgcn_mfma_f32_16x16x32_bf16((a), (b), (c), 0, 0, 0)

constexpr int SEQ = 2048, DM = 640, FF = 1536, NQKV = 1920;

constexpr size_t OFF_HF = 0;
constexpr size_t OFF_QF = OFF_HF + 128ull * 48 * 1024;
constexpr size_t OFF_AF = OFF_QF + 128ull * 20 * 1024;
constexpr size_t OFF_W2F = OFF_AF + 128ull * 20 * 1024;
constexpr size_t OFF_WPF = OFF_W2F + 48ull * 40 * 1024;
constexpr size_t OFF_KVP = OFF_WPF + 20ull * 40 * 1024;
constexpr size_t OFF_ZP = OFF_KVP + 16ull * 32 * 1600;
constexpr size_t OFF_G = OFF_ZP + 16ull * 640 * 4;
constexpr size_t OFF_FLAG = OFF_G + 2048 * 4;
constexpr size_t OFF_DBG = 64ull << 20;
#define STAMP(kern, idx)
#define RSTAMP(kern, idx)
#define LSTAMP(idx)

DI unsigned pack2(float lo, float hi) {
  typedef __bf16 bf2 __attribute__((ext_vector_type(2)));
  typedef float f2 __attribute__((ext_vector_type(2)));
  f2 v = {lo, hi};
  bf2 r = __builtin_convertvector(v, bf2);
  return __builtin_bit_cast(unsigned, r);
}
DI float bf_lo(unsigned u) { return __uint_as_float(u << 16); }
DI float bf_hi(unsigned u) { return __uint_as_float(u & 0xffff0000u); }
DI float rbf(float f) { return bf_lo(pack2(f, 0.f) & 0xffffu); }
DI float phi(float t) {
  float e = __builtin_amdgcn_exp2f(t * 1.4426950408889634f);
  e = fminf(e, 1.0f);
  return fmaxf(t, 0.f) + e + 1e-6f;
}

constexpr int K1_STAGE = 45056;
constexpr int K1_LDS = 3 * K1_STAGE;

struct ColSrc { const u16* p; int stride; };
template <bool KV> DI ColSrc pos_src(int pos, int j, const u16* wqkv, const u16* w1) {
  ColSrc s;
  if (KV) {
    if (pos < 5) { s.p = wqkv + 640 + 80 * j + 16 * pos; s.stride = NQKV; }
    else if (pos == 5) { s.p = wqkv + 16 * (32 + j); s.stride = NQKV; }
    else if (pos < 11) { s.p = wqkv + 1280 + 80 * j + 16 * (pos - 6); s.stride = NQKV; }
    else if (pos == 11) { s.p = wqkv + 1280 + 80 * j + 64; s.stride = NQKV; }
    else { s.p = w1 + 32 * (40 + j) + 16 * (pos - 12); s.stride = FF; }
  } else {
    if (pos < 2) { s.p = wqkv + 32 * (2 * j) + 16 * pos; s.stride = NQKV; }
    else if (pos < 6) { s.p = w1 + 32 * (5 * j) + 16 * (pos - 2); s.stride = FF; }
    else if (pos < 8) { s.p = wqkv + 32 * (2 * j + 1) + 16 * (pos - 6); s.stride = NQKV; }
    else { s.p = w1 + 32 * (5 * j + 2) + 16 * (pos - 8); s.stride = FF; }
  }
  return s;
}
template <bool KV> DI int slot_pos(int wn, int ni) {
  if (KV) {
    const int p0 = ni < 3 ? ni : (ni < 5 ? ni + 3 : ni + 7);
    const int p1 = ni < 2 ? ni + 3 : (ni < 5 ? ni + 6 : (ni == 5 ? 5 : 11));
    return wn ? p1 : p0;
  }
  return ni < 6 ? 6 * wn + ni : 12 + wn;
}
DI float relu_f(float v) { return __int_as_float(max(__float_as_int(v), 0)); }
DI float phi1(float t1) {
  float e = __builtin_amdgcn_exp2f(__builtin_fmaf(t1, 1.4426950408889634f, -1.4426950408889634e-6f));
  e = __builtin_amdgcn_fmed3f(e, 0.0f, 1.0f);
  return __int_as_float(max(__float_as_int(t1), __float_as_int(1e-6f))) + e;
}
DI u32x2 cvt_q(const f32x4& a) { return (u32x2){pack2(phi1(a[0]), phi1(a[1])), pack2(phi1(a[2]), phi1(a[3]))}; }
DI u32x2 cvt_h(const f32x4& a) {
  float t0 = relu_f(a[0]), t1 = relu_f(a[1]), t2 = relu_f(a[2]), t3 = relu_f(a[3]);
  return (u32x2){pack2(t0 * t0, t1 * t1), pack2(t2 * t2, t3 * t3)};
}
DI u32x2 cvt_v(const f32x4& a) { return (u32x2){pack2(a[0], a[1]), pack2(a[2], a[3])}; }

template <bool KV>
DI void k1_body(char* smem, int rb, int j, int ci, int ri, const u16* __restrict__ x, const u16* __restrict__ wqkv,
                const u16* __restrict__ w1, const u16* __restrict__ b1, const u16* __restrict__ w2,
                const u16* __restrict__ gate_w, char* __restrict__ ws) {
  const int tid = threadIdx.x;
  const int lane = tid & 63;
  const int w = __builtin_amdgcn_readfirstlane(tid >> 6);
  const int wm = w >> 1, wn = w & 1;
  const int g = lane >> 4, c = lane & 15;
  RSTAMP(0, 30); STAMP(0, 0);

  const __amdgpu_buffer_rsrc_t rsX = __builtin_amdgcn_make_buffer_rsrc((void*)x, 0, SEQ * DM * 2, 0x00020000);
  const __amdgpu_buffer_rsrc_t rsQ = __builtin_amdgcn_make_buffer_rsrc((void*)wqkv, 0, DM * NQKV * 2, 0x00020000);
  const __amdgpu_buffer_rsrc_t rsW = __builtin_amdgcn_make_buffer_rsrc((void*)w1, 0, DM * FF * 2, 0x00020000);
  const int vA = (lane >> 3) * (DM * 2) + (((lane & 7) ^ ((lane >> 3) & 7)) * 16);
#define NCOM 0
  const int rot = NCOM ? (ri + 2 * ci) % (10 - NCOM) : (ri + (5 * ci) / 2) % 10;
  int ks = NCOM ? 0 : rot, cnt = 0;
  int sA = (rb * 128 + 32 * w) * (DM * 2) + ks * 128;
  const int r16 = lane >> 2;
  const int cs = (lane & 3) ^ (((r16 >> 3) & 1) << 1);
  const int vQ = r16 * (NQKV * 2) + cs * 16;
  const int vW = r16 * (FF * 2) + cs * 16;
  const int vS = r16 * (NQKV * 2) + (cs & 1) * 16 + ((cs >> 1) ? 0 : ((640 + 80 * j + 64) - 16 * (32 + j)) * 2);
  const int vU = r16 * (NQKV * 2) + (cs & 1) * 16;
  int sQ = (w * 16 + ks * 64) * (NQKV * 2), sW = (w * 16 + ks * 64) * (FF * 2);
  auto st1 = [&](int buf, int i) {
    char* base = smem + buf * K1_STAGE;
    typedef __attribute__((address_space(3))) void* ldsp;
    if (i < 4) {
      __builtin_amdgcn_raw_ptr_buffer_load_lds(rsX, (ldsp)(base + (w * 4 + i) * 1024), 16, vA, sA + i * 8 * DM * 2, 0, 0);

    } else {
      const int p = i - 4;
      ldsp dst = (ldsp)(base + 16384 + (w * 7 + p) * 1024);
      const int col = (int)(pos_src<KV>((KV && p == 2) ? 5 : 2 * p, j, (const u16*)0, (const u16*)0).p - (const u16*)0) * 2;
      const bool isW = KV ? (p == 6) : !(p == 0 || p == 3);
      if (isW) __builtin_amdgcn_raw_ptr_buffer_load_lds(rsW, dst, 16, vW, sW + col, 0, 0);
      else __builtin_amdgcn_raw_ptr_buffer_load_lds(rsQ, dst, 16, (KV && p == 2) ? vS : ((KV && p == 5) ? vU : vQ), sQ + col, 0, 0);
      if (p == 6) {
        ++cnt;
        const int adv = cnt == NCOM ? 1 + rot : (ks == 9 ? -(9 - NCOM) : 1);
        ks += adv; sA += adv * 128; sQ += adv * (64 * NQKV * 2); sW += adv * (64 * FF * 2);
      }
    }
  };
  asm volatile("" ::: "memory");
#pragma unroll
  for (int i = 0; i < 11; ++i) st1(0, i);
#pragma unroll
  for (int i = 0; i < 11; ++i) st1(1, i);
  auto extras = [&]() {
    const u16* bsrc = b1 + (KV ? 32 * (40 + j) : 160 * j) + min(lane, KV ? 3 : 19) * 8;
    __builtin_amdgcn_global_load_lds((const unsigned*)bsrc, (unsigned*)(smem + K1_LDS + 1024 + w * 1024), 16, 0, 0);
    const int gwv = min(blockIdx.x * 4 + w, 959), kb = gwv / 20, np = gwv % 20;
#pragma unroll
    for (int i = 0; i < 2; ++i)
      __builtin_amdgcn_global_load_lds((const unsigned*)(w2 + (size_t)(kb * 32 + i * 16 + (lane >> 2)) * DM + np * 32 + (lane & 3) * 8),
                                       (unsigned*)(smem + K1_LDS + 6144 + (w * 2 + i) * 1024), 16, 0, 0);
  };
  asm volatile("" ::: "memory");

  const int aoff0 = (wm * 64 + c) * 128 + ((g ^ (c & 7)) * 16);
  const int aoff1 = (wm * 64 + c) * 128 + (((g + 4) ^ (c & 7)) * 16);
  const int q4 = c >> 2, p4 = c & 3;
  int boff[7];
#pragma unroll
  for (int ni = 0; ni < 7; ++ni) {
    const int pos = slot_pos<KV>(wn, ni);
    boff[ni] = 16384 + (g >> 1) * 7168 + (8 * (g & 1) + q4) * 64 + (p4 & 1) * 8 +
               (((2 * (pos & 1) + (p4 >> 1)) ^ ((g & 1) << 1)) * 16) + (pos >> 1) * 1024;
  }

  f32x4 acc[4][7];
  struct Frags { bf16x8 a[4]; s16x4 bl[7], bh[7]; };
  Frags F0, F1;
  auto rd1 = [&](Frags& F, int buf, int kk, int idx) {
    const char* base = smem + buf * K1_STAGE;
    if (idx < 4) F.a[idx] = *(const bf16x8*)(base + (kk ? aoff1 : aoff0) + idx * 2048);
    else {
      const int ni = (idx - 4) >> 1, hi = (idx - 4) & 1;
      const char* bp = base + boff[ni] + kk * 14336 + hi * 256;
      s16x4 v = __builtin_amdgcn_ds_read_tr16_b64_v4i16((lds_s16x4_ptr)(bp));
      if (hi) F.bh[ni] = v; else F.bl[ni] = v;
    }
  };
  auto rdst = [&](Frags& F, int buf, int kk, int nb, int p0, int np) {
#pragma unroll
    for (int i = 0; i < 6; ++i) {
      rd1(F, buf, kk, 3 * i); rd1(F, buf, kk, 3 * i + 1); rd1(F, buf, kk, 3 * i + 2);
      if (i < np) st1(nb, p0 + i);
    }
  };
  auto mma = [&](Frags& F) {
#pragma unroll
    for (int ni = 0; ni < 7; ++ni) {
      bf16x8 fb = __builtin_shufflevector(F.bl[ni], F.bh[ni], 0, 1, 2, 3, 4, 5, 6, 7);
#pragma unroll
      for (int mi = 0; mi < 4; ++mi) {
        if (KV && ni < 5) acc[mi][ni] = MFMA16(F.a[mi], fb, acc[mi][ni]);
        else acc[mi][ni] = MFMA16(fb, F.a[mi], acc[mi][ni]);
      }
    }
  };

#define K1_INTERLEAVE(NV)                                                   \
  _Pragma("unroll") for (int i_ = 0; i_ < 6; ++i_) {                        \
    __builtin_amdgcn_sched_group_barrier(0x008, 1, 0);                      \
    __builtin_amdgcn_sched_group_barrier(0x100, 1, 0);                      \
    __builtin_amdgcn_sched_group_barrier(0x008, 1, 0);                      \
    __builtin_amdgcn_sched_group_barrier(0x100, 1, 0);                      \
    __builtin_amdgcn_sched_group_barrier(0x008, 1, 0);                      \
    __builtin_amdgcn_sched_group_barrier(0x100, 1, 0);                      \
    if (i_ < (NV)) __builtin_amdgcn_sched_group_barrier(0x010, 1, 0);       \
    __builtin_amdgcn_sched_group_barrier(0x008, 1, 0);                      \
  }                                                                         \
  __builtin_amdgcn_sched_group_barrier(0x008, 4, 0);

  const __amdgpu_buffer_rsrc_t rsO = __builtin_amdgcn_make_buffer_rsrc(ws, 0, (int)OFF_FLAG, 0x00020000);
  asm volatile("s_waitcnt vmcnt(11)" ::: "memory");
  __builtin_amdgcn_s_barrier();
  asm volatile("" ::: "memory");
  STAMP(0, 1);
#pragma unroll
  for (int ni = 0; ni < 7; ++ni) {
    float iv;
    if (KV) iv = ni < 2 ? 1e-6f : (ni == 2 ? (wn ? 0.f : 1e-6f) : (ni == 5 ? (wn ? 1e-6f : 0.f) : 0.f));
    else iv = ni < 2 ? 1e-6f : 0.f;
#pragma unroll
    for (int mi = 0; mi < 4; ++mi) acc[mi][ni] = (f32x4){iv, iv, iv, iv};
  }
  rdst(F0, 0, 0, 2, 0, 6);
  __builtin_amdgcn_sched_barrier(0);
  rdst(F1, 0, 1, 2, 6, 5);
  mma(F0);
  K1_INTERLEAVE(5)
  __builtin_amdgcn_sched_barrier(0);
  extras();
  __builtin_amdgcn_sched_barrier(0);
  int buf = 1, nbuf = 0;
#pragma unroll 1
  for (int s = 1; s < 8; ++s) {
    if (s < 3) asm volatile("s_waitcnt lgkmcnt(0)\n\ts_waitcnt vmcnt(14)" ::: "memory");
    else asm volatile("s_waitcnt lgkmcnt(0)\n\ts_waitcnt vmcnt(11)" ::: "memory");
    LSTAMP(2 * s);
    __builtin_amdgcn_s_barrier();
    asm volatile("" ::: "memory");
    LSTAMP(2 * s + 1);
    __builtin_amdgcn_sched_barrier(0);
    rdst(F0, buf, 0, nbuf, 0, 6);
    mma(F1);
    K1_INTERLEAVE(6)
    __builtin_amdgcn_sched_barrier(0);
    rdst(F1, buf, 1, nbuf, 6, 5);
    mma(F0);
    K1_INTERLEAVE(5)
    __builtin_amdgcn_sched_barrier(0);
    nbuf = buf;
    buf = buf == 2 ? 0 : buf + 1;
  }
  STAMP(0, 2);
  asm volatile("s_waitcnt lgkmcnt(0)\n\ts_waitcnt vmcnt(11)" ::: "memory");
  __builtin_amdgcn_s_barrier();
  asm volatile("" ::: "memory");
  __builtin_amdgcn_sched_barrier(0);
  {
    const int gwv = min(blockIdx.x * 4 + w, 959);
#pragma unroll
    for (int i = 0; i < 2; ++i) {
      const char* bp = smem + K1_LDS + 6144 + w * 2048 + (4 * g + q4) * 64 + i * 32 + p4 * 8;
      s16x4 lo = __builtin_amdgcn_ds_read_tr16_b64_v4i16((lds_s16x4_ptr)(bp));
      s16x4 hi = __builtin_amdgcn_ds_read_tr16_b64_v4i16((lds_s16x4_ptr)(bp + 1024));
      bf16x8 v = __builtin_shufflevector(lo, hi, 0, 1, 2, 3, 4, 5, 6, 7);
      __builtin_amdgcn_raw_buffer_store_b128(__builtin_bit_cast(u32x4, v), rsO, lane * 16 + (int)OFF_W2F + (gwv * 2 + i) * 1024, 0, 16);
    }
  }
  __builtin_amdgcn_sched_barrier(0);
  rdst(F0, 2, 0, 0, 0, 0);
  mma(F1);
  K1_INTERLEAVE(0)
  __builtin_amdgcn_sched_barrier(0);
  rdst(F1, 2, 1, 0, 0, 0);
  mma(F0);
  K1_INTERLEAVE(0)
  __builtin_amdgcn_sched_barrier(0);
  asm volatile("s_waitcnt lgkmcnt(0)\n\ts_waitcnt vmcnt(2)" ::: "memory");
  __builtin_amdgcn_s_barrier();
  asm volatile("" ::: "memory");
  __builtin_amdgcn_sched_barrier(0);
  rdst(F0, 0, 0, 0, 0, 0);
  mma(F1);
  K1_INTERLEAVE(0)
  __builtin_amdgcn_sched_barrier(0);
  rdst(F1, 0, 1, 0, 0, 0);
  mma(F0);
  K1_INTERLEAVE(0)
  __builtin_amdgcn_sched_barrier(0);
  mma(F1);
  STAMP(0, 3);

  STAMP(0, 6);
  const int rt0 = rb * 8 + wm * 4;
#define K1AUX 16
  const int oHF = (int)OFF_HF + rt0 * 48 * 1024, oQF = (int)OFF_QF + rt0 * 20 * 1024;
  auto st16 = [&](int off, u32x4 v) { __builtin_amdgcn_raw_buffer_store_b128(v, rsO, lane * 16 + off, 0, K1AUX); };
  auto st8 = [&](int off, u32x2 v) { __builtin_amdgcn_raw_buffer_store_b64(v, rsO, lane * 16, off, 0); };
  u32x2 bias[7];
  {
    const char* bl = smem + K1_LDS + 1024 + w * 1024 + g * 8;
#pragma unroll
    for (int ni = 0; ni < 7; ++ni) {
      if (KV) { if (ni >= 5) bias[ni] = *(const u32x2*)(bl + 32 * (ni - 5)); }
      else if (ni >= 2) bias[ni] = *(const u32x2*)(bl + (ni < 6 ? 128 * wn + 32 * (ni - 2) : 256 + 32 * wn));
    }
  }
  auto addb = [&](const f32x4& a, const u32x2& b) -> f32x4 {
    return a + (f32x4){bf_lo(b[0]), bf_hi(b[0]), bf_lo(b[1]), bf_hi(b[1])};
  };

  if (!KV) {
    const int grow = rb * 128 + j * 16 + w * 4 + g;
    u32x4 gx[5], gwv[5];
#pragma unroll
    for (int i = 0; i < 5; ++i) {
      gx[i] = *(const u32x4*)(x + (size_t)grow * DM + (i * 16 + c) * 8);
      gwv[i] = *(const u32x4*)(gate_w + (i * 16 + c) * 8);
    }
#pragma unroll
    for (int mi = 0; mi < 4; ++mi) {
      u32x2 lo = cvt_q(acc[mi][0]), hi = cvt_q(acc[mi][1]);
      st16(oQF + (mi * 20 + 2 * j + wn) * 1024, (u32x4){lo[0], lo[1], hi[0], hi[1]});
    }
    STAMP(0, 7);
#pragma unroll
    for (int pr = 0; pr < 2; ++pr)
#pragma unroll
      for (int mi = 0; mi < 4; ++mi) {
        u32x2 lo = cvt_h(addb(acc[mi][2 + 2 * pr], bias[2 + 2 * pr])), hi = cvt_h(addb(acc[mi][3 + 2 * pr], bias[3 + 2 * pr]));
        st16(oHF + (mi * 48 + 5 * j + 2 * wn + pr) * 1024, (u32x4){lo[0], lo[1], hi[0], hi[1]});
      }
    STAMP(0, 8); STAMP(0, 9);
#pragma unroll
    for (int mi = 0; mi < 4; ++mi)
      st8(oHF + (mi * 48 + 5 * j + 4) * 1024 + wn * 8, cvt_h(addb(acc[mi][6], bias[6])));
    {
      typedef __bf16 bf2 __attribute__((ext_vector_type(2)));
      float t0 = 0.f, t1 = 0.f;
#pragma unroll
      for (int i = 0; i < 5; ++i)
#pragma unroll
        for (int e = 0; e < 4; e += 2) {
          const unsigned xa = gx[i][e], wa = gwv[i][e], xb = gx[i][e + 1], wb = gwv[i][e + 1];
          t0 = __builtin_amdgcn_fdot2_f32_bf16(__builtin_bit_cast(bf2, xa), __builtin_bit_cast(bf2, wa), t0, false);
          t1 = __builtin_amdgcn_fdot2_f32_bf16(__builtin_bit_cast(bf2, xb), __builtin_bit_cast(bf2, wb), t1, false);
        }
      float t = t0 + t1;
      t += __shfl_xor(t, 1); t += __shfl_xor(t, 2); t += __shfl_xor(t, 4); t += __shfl_xor(t, 8);
      if (c == 0) ((float*)(ws + OFF_G))[grow] = 1.0f / (1.0f + __expf(-t));
    }
  } else {
    asm volatile("s_waitcnt lgkmcnt(0)" ::: "memory");
    __builtin_amdgcn_s_barrier();
    asm volatile("" ::: "memory");
    STAMP(0, 7);
    char* xk = smem + (wm * 2) * 5 * 1024 + lane * 16;
    char* xv = xk + 20 * 1024;
    if (wn == 0) {
#pragma unroll
      for (int p = 0; p < 2; ++p) {
#pragma unroll
        for (int t = 0; t < 2; ++t) {
          u32x2 lo = cvt_v(acc[2 * p][3 + t]), hi = cvt_v(acc[2 * p + 1][3 + t]);
          *(u32x4*)(xv + (p * 5 + t) * 1024) = (u32x4){lo[0], lo[1], hi[0], hi[1]};
        }
#pragma unroll
        for (int t = 0; t < 3; ++t) {
          u32x2 lo = cvt_q(acc[2 * p][t]), hi = cvt_q(acc[2 * p + 1][t]);
          *(u32x4*)(xk + (p * 5 + t) * 1024) = (u32x4){lo[0], lo[1], hi[0], hi[1]};
        }
      }
    } else {
      *(u32x4*)(smem + 40 * 1024 + lane * 16) = (u32x4){0x3f803f80u, 0x3f803f80u, 0x3f803f80u, 0x3f803f80u};
#pragma unroll
      for (int p = 0; p < 2; ++p) {
#pragma unroll
        for (int t = 0; t < 3; ++t) {
          u32x2 lo = cvt_v(acc[2 * p][2 + t]), hi = cvt_v(acc[2 * p + 1][2 + t]);
          *(u32x4*)(xv + (p * 5 + 2 + t) * 1024) = (u32x4){lo[0], lo[1], hi[0], hi[1]};
        }
#pragma unroll
        for (int t = 0; t < 2; ++t) {
          u32x2 lo = cvt_q(acc[2 * p][t]), hi = cvt_q(acc[2 * p + 1][t]);
          *(u32x4*)(xk + (p * 5 + 3 + t) * 1024) = (u32x4){lo[0], lo[1], hi[0], hi[1]};
        }
      }
    }
    asm volatile("s_waitcnt lgkmcnt(0)" ::: "memory");
    __builtin_amdgcn_s_barrier();
    asm volatile("" ::: "memory");
    STAMP(0, 8);
    const unsigned TE = w == 0 ? 0x54510u : (w == 1 ? 0x55210u : (w == 2 ? 0x55321u : 0x35432u));
    const unsigned TD = w == 0 ? 0x44000u : (w == 1 ? 0x11111u : (w == 2 ? 0x22222u : 0x43333u));
    bf16x8 pk[5][4], vv[5][4];
#pragma unroll
    for (int ch = 0; ch < 5; ++ch) {
      const int te = (TE >> (4 * ch)) & 7, td = (TD >> (4 * ch)) & 7;
      const int vb = te < 5 ? (20 + te) * 1024 : 40 * 1024, vs = te < 5 ? 5 * 1024 : 0;
#pragma unroll
      for (int ks4 = 0; ks4 < 4; ++ks4) {
        pk[ch][ks4] = *(const bf16x8*)(smem + (ks4 * 5 + td) * 1024 + lane * 16);
        vv[ch][ks4] = *(const bf16x8*)(smem + vb + ks4 * vs + lane * 16);
      }
    }
    if (wn == 0) {
#pragma unroll
      for (int mi = 0; mi < 4; ++mi) {
        u32x2 lo = cvt_h(addb(acc[mi][5], bias[5])), hi = cvt_h(addb(acc[mi][6], bias[6]));
        st16(oHF + (mi * 48 + 40 + j) * 1024, (u32x4){lo[0], lo[1], hi[0], hi[1]});
      }
    } else {
#pragma unroll
      for (int mi = 0; mi < 4; ++mi)
        st8(oQF + (mi * 20 + 16 + (j >> 1)) * 1024 + (j & 1) * 8, cvt_q(acc[mi][5]));
    }
    STAMP(0, 9);
    float* KVP = (float*)(ws + OFF_KVP) + (size_t)rb * 32 * 400 + (size_t)j * 4 * 400;
    float* ZP = (float*)(ws + OFF_ZP) + j * 80 * 16 + rb;
#pragma unroll
    for (int ch = 0; ch < 5; ++ch) {
      const int te = (TE >> (4 * ch)) & 7, td = (TD >> (4 * ch)) & 7;
      f32x4 d = {0.f, 0.f, 0.f, 0.f};
#pragma unroll
      for (int ks4 = 0; ks4 < 4; ++ks4) d = MFMA16(vv[ch][ks4], pk[ch][ks4], d);
      const int dg = 16 * td + c;
      if (te < 5) {
        const int eg = 16 * te + 4 * g;
        const int hd = dg / 20, he = eg / 20;
        if (hd == he) *(f32x4*)(KVP + hd * 400 + (dg - hd * 20) * 20 + (eg - he * 20)) = d;
      } else if (g == 0) {
        ZP[dg * 16] = d[0];
      }
    }
  }
}

__global__ __launch_bounds__(256, 2) void k1_kernel(const u16* __restrict__ x, const u16* __restrict__ wqkv,
                                                   const u16* __restrict__ w1, const u16* __restrict__ b1,
                                                   const u16* __restrict__ w2, const u16* __restrict__ gate_w,
                                                   char* __restrict__ ws) {
  extern __shared__ __attribute__((aligned(16))) char smem[];
  const int bid = blockIdx.x;
  const int xcd = bid & 7, idx = bid >> 3;
  const int ri = idx & 7, ci = idx >> 3;
  const int rb = (xcd & 1) * 8 + ri;
  const int cq = (xcd >> 1) * 4 + ci;
  if (bid == 0 && threadIdx.x < 64) ((unsigned*)(ws + OFF_FLAG))[threadIdx.x * 64] = 0u;
  if (idx < 5 && threadIdx.x < 64) {
    const char* pc = (const char*)(__builtin_amdgcn_s_getpc() & ~255ull);
    __builtin_amdgcn_global_load_lds((const unsigned*)(pc + min(idx * 64 + (int)threadIdx.x, 295) * 128), (unsigned*)(smem + K1_LDS), 4, 0, 0);
  }
  if (cq & 1) k1_body<false>(smem, rb, cq >> 1, ci, ri, x, wqkv, w1, b1, w2, gate_w, ws);
  else k1_body<true>(smem, rb, cq >> 1, ci, ri, x, wqkv, w1, b1, w2, gate_w, ws);
  STAMP(0, 4);
  STAMP(0, 5); RSTAMP(0, 31);
}

typedef __attribute__((address_space(1))) unsigned gu32;
#define RLX_AGENT __ATOMIC_RELAXED, __HIP_MEMORY_SCOPE_AGENT
DI void publish(char* ws, int counter, int lane) {
  asm volatile("s_waitcnt vmcnt(0)" ::: "memory");
  if (lane == 0) __hip_atomic_fetch_add((gu32*)(ws + OFF_FLAG) + counter * 64, 1u, RLX_AGENT);
}
DI void task_w(int u, int lane, char* lds  , const u16* __restrict__ wo, char* ws) {
  const __amdgpu_buffer_rsrc_t rs = __builtin_amdgcn_make_buffer_rsrc(ws, 0, (int)OFF_FLAG, 0x00020000);
  const int h = u >> 3, cr = u & 7;
  const int g = lane >> 4, c = lane & 15;
  const float* KVP = (const float*)(ws + OFF_KVP) + h * 400;
  f32x4 k0[16], k1[16];
#pragma unroll
  for (int p = 0; p < 16; ++p) {
    k0[p] = *(const f32x4*)(KVP + (size_t)p * 32 * 400 + lane * 4);
    k1[p] = *(const f32x4*)(KVP + (size_t)p * 32 * 400 + 256 + min(lane, 35) * 4);
  }
#pragma unroll
  for (int i = 0; i < 4; ++i) {
    const int q = min(i * 64 + lane, 199), row = q / 10, ch = q - row * 10;
    __builtin_amdgcn_global_load_lds((const unsigned*)(wo + (size_t)(h * 20 + row) * DM + cr * 80 + ch * 8),
                                     (unsigned*)(lds + 2048 + i * 1024), 16, 0, 0);
  }
  __builtin_amdgcn_sched_barrier(0);
  __builtin_amdgcn_s_barrier();
  __builtin_amdgcn_sched_barrier(0);
  STAMP(2, 8);
  f32x4 s0 = k0[0], s1 = k1[0];
#pragma unroll
  for (int p = 1; p < 16; ++p) { s0 += k0[p]; s1 += k1[p]; }
  asm volatile("s_waitcnt vmcnt(0)" ::: "memory");
  STAMP(2, 9);
  bf16x8 wf[5];
  {
    const int r0 = min(8 * g + (c >> 2), 19), r1 = min(8 * g + 4 + (c >> 2), 19);
#pragma unroll
    for (int t = 0; t < 5; ++t) {
      s16x4 lo = __builtin_amdgcn_ds_read_tr16_b64_v4i16((lds_s16x4_ptr)(lds + 2048 + r0 * 160 + t * 32 + (c & 3) * 8));
      s16x4 hi = __builtin_amdgcn_ds_read_tr16_b64_v4i16((lds_s16x4_ptr)(lds + 2048 + r1 * 160 + t * 32 + (c & 3) * 8));
      wf[t] = __builtin_shufflevector(lo, hi, 0, 1, 2, 3, 4, 5, 6, 7);
    }
  }
  *(u32x4*)(lds + lane * 32) = (u32x4){0u, 0u, 0u, 0u};
  *(u32x4*)(lds + lane * 32 + 16) = (u32x4){0u, 0u, 0u, 0u};
  __builtin_amdgcn_s_waitcnt(0xC07F);
  {
    int i0 = lane * 4, d0 = i0 / 20, e0 = i0 - d0 * 20;
    *(u32x2*)(lds + d0 * 64 + e0 * 2) = (u32x2){pack2(s0[0], s0[1]), pack2(s0[2], s0[3])};
    if (lane < 36) {
      int i1 = 256 + lane * 4, d1 = i1 / 20, e1 = i1 - d1 * 20;
      *(u32x2*)(lds + d1 * 64 + e1 * 2) = (u32x2){pack2(s1[0], s1[1]), pack2(s1[2], s1[3])};
    }
  }
  __builtin_amdgcn_s_waitcnt(0xC07F);
  bf16x8 ka0 = *(const bf16x8*)(lds + c * 64 + g * 16);
  bf16x8 ka1 = *(const bf16x8*)(lds + (16 + c) * 64 + g * 16);
#pragma unroll
  for (int t = 0; t < 5; ++t) {
    f32x4 d0 = MFMA16(ka0, wf[t], ((f32x4){0.f, 0.f, 0.f, 0.f}));
    f32x4 d1 = MFMA16(ka1, wf[t], ((f32x4){0.f, 0.f, 0.f, 0.f}));
    {
      const int k = h * 20 + 4 * g;
      const int kb = k >> 5, half = (k >> 4) & 1, gg = (k & 15) >> 2;
      __builtin_amdgcn_raw_buffer_store_b64((u32x2){pack2(d0[0], d0[1]), pack2(d0[2], d0[3])}, rs,
          (int)OFF_WPF + (kb * 40 + cr * 5 + t) * 1024 + half * 512 + (gg * 16 + c) * 8, 0, 16);
    }
    if (g == 0) {
      const int k = h * 20 + 16;
      const int kb = k >> 5, half = (k >> 4) & 1, gg = (k & 15) >> 2;
      __builtin_amdgcn_raw_buffer_store_b64((u32x2){pack2(d1[0], d1[1]), pack2(d1[2], d1[3])}, rs,
          (int)OFF_WPF + (kb * 40 + cr * 5 + t) * 1024 + half * 512 + (gg * 16 + c) * 8, 0, 16);
    }
  }
  STAMP(2, 10);
  publish(ws, 32 + cr, lane);
}

DI void task_a(int u, int lane, char* lds  , char* ws) {
  const __amdgpu_buffer_rsrc_t rs = __builtin_amdgcn_make_buffer_rsrc(ws, 0, (int)OFF_FLAG, 0x00020000);
  const int rt = u >> 1, kh = u & 1;
  const int g = lane >> 4, c = lane & 15;
  const float* ZP = (const float*)(ws + OFF_ZP) + kh * 320 * 16;
  f32x4 zp[20];
#pragma unroll
  for (int i = 0; i < 20; ++i) zp[i] = *(const f32x4*)(ZP + i * 256 + lane * 4);
  const char* QF = ws + OFF_QF + ((size_t)rt * 20 + kh * 10) * 1024;
  u32x4 qf[10];
#pragma unroll
  for (int b = 0; b < 10; ++b) qf[b] = *(const u32x4*)(QF + b * 1024 + lane * 16);
  const float gate = ((const float*)(ws + OFF_G))[rt * 16 + c];
  __builtin_amdgcn_sched_barrier(0);
  __builtin_amdgcn_s_barrier();
  __builtin_amdgcn_sched_barrier(0);
  STAMP(2, 8);
#pragma unroll
  for (int i = 0; i < 20; ++i) {
    float t = (zp[i][0] + zp[i][1]) + (zp[i][2] + zp[i][3]);
    t += __builtin_bit_cast(float, __builtin_amdgcn_update_dpp(0, __builtin_bit_cast(int, t), 0xB1, 0xF, 0xF, true));
    t += __builtin_bit_cast(float, __builtin_amdgcn_update_dpp(0, __builtin_bit_cast(int, t), 0x4E, 0xF, 0xF, true));
    if ((lane & 3) == 0) *(u16*)(lds + (i * 16 + (lane >> 2)) * 2) = (u16)(pack2(t, 0.f) & 0xffffu);
  }
  STAMP(2, 9);
  __builtin_amdgcn_s_waitcnt(0xC07F);
  f32x4 den = {0.f, 0.f, 0.f, 0.f};
#pragma unroll
  for (int b = 0; b < 10; ++b) {
    const int k0 = 32 * b + 4 * g, k1 = k0 + 16;
    u32x2 zl = *(const u32x2*)(lds + k0 * 2), zh = *(const u32x2*)(lds + k1 * 2);
    const bool m0 = (k0 / 20) == c, m1 = (k1 / 20) == c;
    u32x4 zf = {m0 ? zl[0] : 0u, m0 ? zl[1] : 0u, m1 ? zh[0] : 0u, m1 ? zh[1] : 0u};
    den = MFMA16(__builtin_bit_cast(bf16x8, zf), __builtin_bit_cast(bf16x8, qf[b]), den);
  }
  f32x4 rd;
#pragma unroll
  for (int r = 0; r < 4; ++r) rd[r] = gate / (den[r] + 1e-6f);
  *(f32x4*)(lds + 1024 + c * 64 + g * 16) = rd;
  __builtin_amdgcn_s_waitcnt(0xC07F);
#pragma unroll
  for (int b = 0; b < 10; ++b) {
    const int k0 = 32 * b + 4 * g, k1 = k0 + 16;
    const float r0 = *(const float*)(lds + 1024 + c * 64 + (k0 / 20) * 4);
    const float r1 = *(const float*)(lds + 1024 + c * 64 + (k1 / 20) * 4);
    u32x4 o;
    o[0] = pack2(bf_lo(qf[b][0]) * r0, bf_hi(qf[b][0]) * r0);
    o[1] = pack2(bf_lo(qf[b][1]) * r0, bf_hi(qf[b][1]) * r0);
    o[2] = pack2(bf_lo(qf[b][2]) * r1, bf_hi(qf[b][2]) * r1);
    o[3] = pack2(bf_lo(qf[b][3]) * r1, bf_hi(qf[b][3]) * r1);
    __builtin_amdgcn_raw_buffer_store_b128(o, rs, (int)OFF_AF + (rt * 20 + kh * 10 + b) * 1024 + lane * 16, 0, 16);
  }
  STAMP(2, 10);
  publish(ws, rt >> 2, lane);
}

constexpr int K2_DEPTH = 3;
__global__ __launch_bounds__(384, 2) void k2_kernel(const u16* __restrict__ wo, const u16* __restrict__ b2,
                                                   char* ws, u16* __restrict__ out) {
  __shared__ __attribute__((aligned(16))) char red[4 * 20 * 1024 + 8192 + 16];
  typedef __attribute__((address_space(3))) unsigned lu32;
  lu32* ready = (lu32*)(red + 81920 + 8192);
  const int bid = blockIdx.x;
  const int xcd = bid & 7, idx = bid >> 3;
  const int rb = (xcd >> 1) * 8 + (idx & 7);
  const int cb = (xcd & 1) * 4 + (idx >> 3);
  const int lane = threadIdx.x & 63;
  const int w = __builtin_amdgcn_readfirstlane(threadIdx.x >> 6);
  const int g = lane >> 4, c = lane & 15;

  if (w >= 4) {
    __builtin_amdgcn_s_setprio(3);
    RSTAMP(2, 30); STAMP(2, 0);
    if (w == 4) {
      if (lane < 2) __hip_atomic_store(ready + lane, 0u, __ATOMIC_RELAXED, __HIP_MEMORY_SCOPE_WORKGROUP);
      asm volatile("s_waitcnt lgkmcnt(0)" ::: "memory");
      task_w(bid, lane, red + 81920, wo, ws);
      STAMP(2, 1);
    } else {
      task_a(bid, lane, red + 81920 + 6144, ws);
      STAMP(2, 1);
      gu32* cntA = (gu32*)(ws + OFF_FLAG) + rb * 64;
      gu32* cntW = (gu32*)(ws + OFF_FLAG) + (32 + cb) * 64;
      for (unsigned spins = 0; spins < (1u << 24); ++spins) {
        if (__builtin_amdgcn_readfirstlane(__hip_atomic_load(ready + 1, __ATOMIC_RELAXED, __HIP_MEMORY_SCOPE_WORKGROUP)) != 0u) break;
        __builtin_amdgcn_s_sleep(2);
      }
      for (unsigned spins = 0; spins < (1u << 22); ++spins) {
        const unsigned a = __hip_atomic_load(cntA, RLX_AGENT), b = __hip_atomic_load(cntW, RLX_AGENT);
        if (__builtin_amdgcn_readfirstlane(a) == 8u && __builtin_amdgcn_readfirstlane(b) == 32u) break;
        __builtin_amdgcn_s_sleep(4);
      }
      if (lane == 0) __hip_atomic_store(ready, 1u, __ATOMIC_RELAXED, __HIP_MEMORY_SCOPE_WORKGROUP);
    }
    STAMP(2, 2); STAMP(2, 3); STAMP(2, 4); STAMP(2, 5); RSTAMP(2, 31);
    return;
  }

  const __amdgpu_buffer_rsrc_t rs = __builtin_amdgcn_make_buffer_rsrc(ws, 0, (int)OFF_FLAG, 0x00020000);
  const int voff = lane * 16;
  const int oH = (int)OFF_HF + (rb * 4 * 48 + w) * 1024;
  const int oA = (int)OFF_AF + (rb * 4 * 20 + w) * 1024;
  const int oW2 = (int)OFF_W2F + (w * 40 + cb * 5) * 1024;
  const int oWP = (int)OFF_WPF + (w * 40 + cb * 5) * 1024;

  RSTAMP(2, 30); STAMP(2, 0);
  f32x4 acc[4][5];
#pragma unroll
  for (int mi = 0; mi < 4; ++mi)
#pragma unroll
    for (int ni = 0; ni < 5; ++ni) acc[mi][ni] = (f32x4){0.f, 0.f, 0.f, 0.f};

  constexpr int NS = K2_DEPTH + 1;
  u32x4 fa[NS][4], fb[NS][5];
  float gv[4];
  u32x2 b2v[5];
#define K2ROTR 0
#define K2ROTC 0
  const int rot = (((idx & 7) * K2ROTR) / 2 + ((idx >> 3) * K2ROTC) / 2) % 12;
  auto load = [&](int i, int slot) {
    if (i < 12) {
      int ir = i + rot; ir = ir >= 12 ? ir - 12 : ir;
#pragma unroll
      for (int ni = 0; ni < 5; ++ni) fb[slot][ni] = __builtin_amdgcn_raw_buffer_load_b128(rs, voff, oW2 + (4 * ir * 40 + ni) * 1024, 0);
#pragma unroll
      for (int mi = 0; mi < 4; ++mi) fa[slot][mi] = __builtin_amdgcn_raw_buffer_load_b128(rs, voff, oH + (mi * 48 + 4 * ir) * 1024, 0);
    } else {
      const int ii = i - 12;
#pragma unroll
      for (int ni = 0; ni < 5; ++ni) {
        u32x2 lo = __builtin_amdgcn_raw_buffer_load_b64(rs, lane * 8, oWP + (4 * ii * 40 + ni) * 1024, 16);
        u32x2 hi = __builtin_amdgcn_raw_buffer_load_b64(rs, lane * 8, oWP + (4 * ii * 40 + ni) * 1024 + 512, 16);
        fb[slot][ni] = (u32x4){lo[0], lo[1], hi[0], hi[1]};
      }
#pragma unroll
      for (int mi = 0; mi < 4; ++mi) fa[slot][mi] = __builtin_amdgcn_raw_buffer_load_b128(rs, voff, oA + (mi * 20 + 4 * ii) * 1024, 16);
    }
  };
  __builtin_amdgcn_s_barrier();
  __builtin_amdgcn_sched_barrier(0);
#pragma unroll
  for (int d = 0; d < K2_DEPTH; ++d) load(d, d);
#pragma unroll
  for (int mi = 0; mi < 4; ++mi)
    gv[mi] = __uint_as_float(__builtin_amdgcn_raw_buffer_load_b32(rs, c * 4, (int)OFF_G + (rb * 64 + mi * 16) * 4, 0));
  __builtin_amdgcn_sched_barrier(0);
#pragma unroll
  for (int i = 0; i < 17; ++i) {
    const int slot = i % NS;
    if (i == 1) { STAMP(2, 1); }
#define K2GO 6
    if (i == K2GO) {
      if (lane == 0) __hip_atomic_store(ready + 1, 1u, __ATOMIC_RELAXED, __HIP_MEMORY_SCOPE_WORKGROUP);
      __builtin_amdgcn_sched_barrier(0);
    }
    if (i == 12 - K2_DEPTH) {
      STAMP(2, 6);
      for (unsigned spins = 0; spins < (1u << 24); ++spins) {
        if (__builtin_amdgcn_readfirstlane(__hip_atomic_load(ready, __ATOMIC_RELAXED, __HIP_MEMORY_SCOPE_WORKGROUP)) != 0u) break;
        __builtin_amdgcn_s_sleep(1);
      }
      asm volatile("" ::: "memory");
      STAMP(2, 7);
      __builtin_amdgcn_sched_barrier(0);
    }
    if (i == 12) {
      STAMP(2, 2);
#pragma unroll
      for (int mi = 0; mi < 4; ++mi) {
        gv[mi] = 1.0f - gv[mi];
#pragma unroll
        for (int ni = 0; ni < 5; ++ni) acc[mi][ni] *= gv[mi];
      }
      __builtin_amdgcn_sched_barrier(0);
    }
    if (i + K2_DEPTH < 17) load(i + K2_DEPTH, (i + K2_DEPTH) % NS);
    if (i == 15) {
#pragma unroll
      for (int ni = 0; ni < 5; ++ni) b2v[ni] = *(const u32x2*)(b2 + cb * 80 + ni * 16 + 4 * g);
      __builtin_amdgcn_sched_barrier(0);
    }
#pragma unroll
    for (int mi = 0; mi < 4; ++mi)
#pragma unroll
      for (int ni = 0; ni < 5; ++ni)
        acc[mi][ni] = MFMA16(__builtin_bit_cast(bf16x8, fb[slot][ni]), __builtin_bit_cast(bf16x8, fa[slot][mi]), acc[mi][ni]);
    if (i + K2_DEPTH < 12) {
#pragma unroll
      for (int k = 0; k < 9; ++k) {
        __builtin_amdgcn_sched_group_barrier(0x008, 2, 0);
        __builtin_amdgcn_sched_group_barrier(0x020, 1, 0);
      }
      __builtin_amdgcn_sched_group_barrier(0x008, 2, 0);
    } else if (i + K2_DEPTH < 17) {
#pragma unroll
      for (int k = 0; k < 14; ++k) {
        __builtin_amdgcn_sched_group_barrier(0x008, 1, 0);
        __builtin_amdgcn_sched_group_barrier(0x020, 1, 0);
      }
      __builtin_amdgcn_sched_group_barrier(0x008, 6, 0);
    }
    __builtin_amdgcn_sched_barrier(0);
  }

  STAMP(2, 3);
#pragma unroll
  for (int mi = 0; mi < 4; ++mi)
    if (mi != w) {
#pragma unroll
      for (int ni = 0; ni < 5; ++ni) *(f32x4*)(red + ((w * 4 + mi) * 5 + ni) * 1024 + lane * 16) = acc[mi][ni];
    }
  __syncthreads();
  float gw = gv[0];
  f32x4 mine[5];
#pragma unroll
  for (int ni = 0; ni < 5; ++ni) mine[ni] = acc[0][ni];
#pragma unroll
  for (int mi = 1; mi < 4; ++mi)
    if (w == mi) {
      gw = gv[mi];
#pragma unroll
      for (int ni = 0; ni < 5; ++ni) mine[ni] = acc[mi][ni];
    }
#pragma unroll
  for (int ni = 0; ni < 5; ++ni) {
    f32x4 s = mine[ni];
#pragma unroll
    for (int o = 1; o < 4; ++o) {
      const int ow = (w + o) & 3;
      s += *(const f32x4*)(red + ((ow * 4 + w) * 5 + ni) * 1024 + lane * 16);
    }
    const int n = cb * 80 + ni * 16 + 4 * g;
    const u32x2 bv = b2v[ni];
    s[0] += gw * bf_lo(bv[0]); s[1] += gw * bf_hi(bv[0]); s[2] += gw * bf_lo(bv[1]); s[3] += gw * bf_hi(bv[1]);
    *(u32x2*)(out + (size_t)(rb * 64 + w * 16 + c) * DM + n) = (u32x2){pack2(s[0], s[1]), pack2(s[2], s[3])};
  }
  STAMP(2, 4);
  STAMP(2, 5); RSTAMP(2, 31);
}

extern "C" void kernel_launch(void* const* d_in, const int* in_sizes, int n_in, void* d_out, int out_size,
                              void* d_ws, size_t ws_size, hipStream_t stream) {
  const u16* x = (const u16*)d_in[0];
  const u16* wqkv = (const u16*)d_in[1];
  const u16* wo = (const u16*)d_in[2];
  const u16* w1 = (const u16*)d_in[4];
  const u16* b1 = (const u16*)d_in[5];
  const u16* w2 = (const u16*)d_in[6];
  const u16* b2 = (const u16*)d_in[7];
  const u16* gw = (const u16*)d_in[8];
  char* ws = (char*)d_ws;
  (void)hipFuncSetAttribute((const void*)k1_kernel, hipFuncAttributeMaxDynamicSharedMemorySize, K1_LDS + 14336);
  k1_kernel<<<256, 256, K1_LDS + 14336, stream>>>(x, wqkv, w1, b1, w2, gw, ws);
  k2_kernel<<<256, 384, 0, stream>>>(wo, b2, ws, (u16*)d_out);
}
```

```cpp
#include <hip/hip_runtime.h>
#include <hip/hip_bf16.h>

typedef unsigned short u16;
typedef short bf16x8 __attribute__((ext_vector_type(8)));
typedef short s16x4 __attribute__((ext_vector_type(4)));
typedef float f32x4 __attribute__((ext_vector_type(4)));
typedef unsigned u32x4 __attribute__((ext_vector_type(4)));
typedef unsigned u32x2 __attribute__((ext_vector_type(2)));
typedef __attribute__((address_space(3))) s16x4* lds_s16x4_ptr;
#define DI __device__ __forceinline__
#define MFMA16(a, b, c) __builtin_amdgcn_mfma_f32_16x16x32_bf16((a), (b), (c), 0, 0, 0)

constexpr int SEQ = 2048, DM = 640, FF = 1536, NQKV = 1920;

constexpr size_t OFF_HF = 0;
constexpr size_t OFF_QF = OFF_HF + 128ull * 48 * 1024;
constexpr size_t OFF_AF = OFF_QF + 128ull * 20 * 1024;
constexpr size_t OFF_W2F = OFF_AF + 128ull * 20 * 1024;
constexpr size_t OFF_WPF = OFF_W2F + 48ull * 40 * 1024;
constexpr size_t OFF_KVP = OFF_WPF + 20ull * 40 * 1024;
constexpr size_t OFF_ZP = OFF_KVP + 16ull * 32 * 1600;
constexpr size_t OFF_G = OFF_ZP + 16ull * 640 * 4;
constexpr size_t OFF_FLAG = OFF_G + 2048 * 4;
constexpr size_t OFF_DBG = 64ull << 20;
#define STAMP(kern, idx)
#define RSTAMP(kern, idx)
#define LSTAMP(idx)

DI unsigned pack2(float lo, float hi) {
  typedef __bf16 bf2 __attribute__((ext_vector_type(2)));
  typedef float f2 __attribute__((ext_vector_type(2)));
  f2 v = {lo, hi};
  bf2 r = __builtin_convertvector(v, bf2);
  return __builtin_bit_cast(unsigned, r);
}
DI float bf_lo(unsigned u) { return __uint_as_float(u << 16); }
DI float bf_hi(unsigned u) { return __uint_as_float(u & 0xffff0000u); }
DI float rbf(float f) { return bf_lo(pack2(f, 0.f) & 0xffffu); }
DI float phi(float t) {
  float e = __builtin_amdgcn_exp2f(t * 1.4426950408889634f);
  e = fminf(e, 1.0f);
  return fmaxf(t, 0.f) + e + 1e-6f;
}

constexpr int K1_STAGE = 45056;
constexpr int K1_LDS = 3 * K1_STAGE;

struct ColSrc { const u16* p; int stride; };
template <bool KV> DI ColSrc pos_src(int pos, int j, const u16* wqkv, const u16* w1) {
  ColSrc s;
  if (KV) {
    if (pos < 5) { s.p = wqkv + 640 + 80 * j + 16 * pos; s.stride = NQKV; }
    else if (pos == 5) { s.p = wqkv + 16 * (32 + j); s.stride = NQKV; }
    else if (pos < 11) { s.p = wqkv + 1280 + 80 * j + 16 * (pos - 6); s.stride = NQKV; }
    else if (pos == 11) { s.p = wqkv + 1280 + 80 * j + 64; s.stride = NQKV; }
    else { s.p = w1 + 32 * (40 + j) + 16 * (pos - 12); s.stride = FF; }
  } else {
    if (pos < 2) { s.p = wqkv + 32 * (2 * j) + 16 * pos; s.stride = NQKV; }
    else if (pos < 6) { s.p = w1 + 32 * (5 * j) + 16 * (pos - 2); s.stride = FF; }
    else if (pos < 8) { s.p = wqkv + 32 * (2 * j + 1) + 16 * (pos - 6); s.stride = NQKV; }
    else { s.p = w1 + 32 * (5 * j + 2) + 16 * (pos - 8); s.stride = FF; }
  }
  return s;
}
template <bool KV> DI int slot_pos(int wn, int ni) {
  if (KV) {
    const int p0 = ni < 3 ? ni : (ni < 5 ? ni + 3 : ni + 7);
    const int p1 = ni < 2 ? ni + 3 : (ni < 5 ? ni + 6 : (ni == 5 ? 5 : 11));
    return wn ? p1 : p0;
  }
  return ni < 6 ? 6 * wn + ni : 12 + wn;
}
DI float relu_f(float v) { return __int_as_float(max(__float_as_int(v), 0)); }
DI float phi1(float t1) {
  float e = __builtin_amdgcn_exp2f(__builtin_fmaf(t1, 1.4426950408889634f, -1.4426950408889634e-6f));
  e = __builtin_amdgcn_fmed3f(e, 0.0f, 1.0f);
  return __int_as_float(max(__float_as_int(t1), __float_as_int(1e-6f))) + e;
}
DI u32x2 cvt_q(const f32x4& a) { return (u32x2){pack2(phi1(a[0]), phi1(a[1])), pack2(phi1(a[2]), phi1(a[3]))}; }
DI u32x2 cvt_h(const f32x4& a) {
  float t0 = relu_f(a[0]), t1 = relu_f(a[1]), t2 = relu_f(a[2]), t3 = relu_f(a[3]);
  return (u32x2){pack2(t0 * t0, t1 * t1), pack2(t2 * t2, t3 * t3)};
}
DI u32x2 cvt_v(const f32x4& a) { return (u32x2){pack2(a[0], a[1]), pack2(a[2], a[3])}; }

template <bool KV>
DI void k1_body(char* smem, int rb, int j, int ci, int ri, const u16* __restrict__ x, const u16* __restrict__ wqkv,
                const u16* __restrict__ w1, const u16* __restrict__ b1, const u16* __restrict__ w2,
                const u16* __restrict__ gate_w, char* __restrict__ ws) {
  const int tid = threadIdx.x;
  const int lane = tid & 63;
  const int w = __builtin_amdgcn_readfirstlane(tid >> 6);
  const int wm = w >> 1, wn = w & 1;
  const int g = lane >> 4, c = lane & 15;
  RSTAMP(0, 30); STAMP(0, 0);

  const __amdgpu_buffer_rsrc_t rsX = __builtin_amdgcn_make_buffer_rsrc((void*)x, 0, SEQ * DM * 2, 0x00020000);
  const __amdgpu_buffer_rsrc_t rsQ = __builtin_amdgcn_make_buffer_rsrc((void*)wqkv, 0, DM * NQKV * 2, 0x00020000);
  const __amdgpu_buffer_rsrc_t rsW = __builtin_amdgcn_make_buffer_rsrc((void*)w1, 0, DM * FF * 2, 0x00020000);
  const int vA = (lane >> 3) * (DM * 2) + (((lane & 7) ^ ((lane >> 3) & 7)) * 16);
#define NCOM 2
  const int rot = NCOM ? (ri + 2 * ci) % (10 - NCOM) : (ri + (5 * ci) / 2) % 10;
  int ks = NCOM ? 0 : rot, cnt = 0;
  int sA = (rb * 128 + 32 * w) * (DM * 2) + ks * 128;
  const int r16 = lane >> 2;
  const int cs = (lane & 3) ^ (((r16 >> 3) & 1) << 1);
  const int vQ = r16 * (NQKV * 2) + cs * 16;
  const int vW = r16 * (FF * 2) + cs * 16;
  const int vS = r16 * (NQKV * 2) + (cs & 1) * 16 + ((cs >> 1) ? 0 : ((640 + 80 * j + 64) - 16 * (32 + j)) * 2);
  const int vU = r16 * (NQKV * 2) + (cs & 1) * 16;
  int sQ = (w * 16 + ks * 64) * (NQKV * 2), sW = (w * 16 + ks * 64) * (FF * 2);
  auto st1 = [&](int buf, int i) {
    char* base = smem + buf * K1_STAGE;
    typedef __attribute__((address_space(3))) void* ldsp;
    if (i < 4) {
      __builtin_amdgcn_raw_ptr_buffer_load_lds(rsX, (ldsp)(base + (w * 4 + i) * 1024), 16, vA, sA + i * 8 * DM * 2, 0, 0);

    } else {
      const int p = i - 4;
      ldsp dst = (ldsp)(base + 16384 + (w * 7 + p) * 1024);
      const int col = (int)(pos_src<KV>((KV && p == 2) ? 5 : 2 * p, j, (const u16*)0, (const u16*)0).p - (const u16*)0) * 2;
      const bool isW = KV ? (p == 6) : !(p == 0 || p == 3);
      if (isW) __builtin_amdgcn_raw_ptr_buffer_load_lds(rsW, dst, 16, vW, sW + col, 0, 0);
      else __builtin_amdgcn_raw_ptr_buffer_load_lds(rsQ, dst, 16, (KV && p == 2) ? vS : ((KV && p == 5) ? vU : vQ), sQ + col, 0, 0);
      if (p == 6) {
        ++cnt;
        const int adv = cnt == NCOM ? 1 + rot : (ks == 9 ? -(9 - NCOM) : 1);
        ks += adv; sA += adv * 128; sQ += adv * (64 * NQKV * 2); sW += adv * (64 * FF * 2);
      }
    }
  };
  asm volatile("" ::: "memory");
  const int grow = rb * 128 + j * 16 + w * 4 + g;
  u32x4 gx[5], gwv[5];
  if (!KV) {
    const u16* xp = x + (size_t)grow * DM + c * 8;
    const u16* gp = gate_w + c * 8;
#pragma unroll
    for (int i = 0; i < 5; ++i) {
      asm volatile("global_load_dwordx4 %0, %1, off offset:%2" : "=&v"(gx[i]) : "v"(xp), "n"(i * 256) : "memory");
      asm volatile("global_load_dwordx4 %0, %1, off offset:%2" : "=&v"(gwv[i]) : "v"(gp), "n"(i * 256) : "memory");
    }
  }
#pragma unroll
  for (int i = 0; i < 11; ++i) st1(0, i);
#pragma unroll
  for (int i = 0; i < 11; ++i) st1(1, i);
  if (!KV) {
    asm volatile("s_waitcnt vmcnt(22)"
                 : "+v"(gx[0]), "+v"(gx[1]), "+v"(gx[2]), "+v"(gx[3]), "+v"(gx[4]),
                   "+v"(gwv[0]), "+v"(gwv[1]), "+v"(gwv[2]), "+v"(gwv[3]), "+v"(gwv[4]) :: "memory");
    typedef __bf16 bf2 __attribute__((ext_vector_type(2)));
    float t0 = 0.f, t1 = 0.f;
#pragma unroll
    for (int i = 0; i < 5; ++i)
#pragma unroll
      for (int e = 0; e < 4; e += 2) {
        const unsigned xa = gx[i][e], wa = gwv[i][e], xb = gx[i][e + 1], wb = gwv[i][e + 1];
        t0 = __builtin_amdgcn_fdot2_f32_bf16(__builtin_bit_cast(bf2, xa), __builtin_bit_cast(bf2, wa), t0, false);
        t1 = __builtin_amdgcn_fdot2_f32_bf16(__builtin_bit_cast(bf2, xb), __builtin_bit_cast(bf2, wb), t1, false);
      }
    float t = t0 + t1;
    t += __shfl_xor(t, 1); t += __shfl_xor(t, 2); t += __shfl_xor(t, 4); t += __shfl_xor(t, 8);
    ((float*)(ws + OFF_G))[grow] = 1.0f / (1.0f + __expf(-t));
  }
  auto extras = [&]() {
    const u16* bsrc = b1 + (KV ? 32 * (40 + j) : 160 * j) + min(lane, KV ? 3 : 19) * 8;
    __builtin_amdgcn_global_load_lds((const unsigned*)bsrc, (unsigned*)(smem + K1_LDS + 1024 + w * 1024), 16, 0, 0);
    const int gwv = min(blockIdx.x * 4 + w, 959), kb = gwv / 20, np = gwv % 20;
#pragma unroll
    for (int i = 0; i < 2; ++i)
      __builtin_amdgcn_global_load_lds((const unsigned*)(w2 + (size_t)(kb * 32 + i * 16 + (lane >> 2)) * DM + np * 32 + (lane & 3) * 8),
                                       (unsigned*)(smem + K1_LDS + 6144 + (w * 2 + i) * 1024), 16, 0, 0);
  };
  asm volatile("" ::: "memory");

  const int aoff0 = (wm * 64 + c) * 128 + ((g ^ (c & 7)) * 16);
  const int aoff1 = (wm * 64 + c) * 128 + (((g + 4) ^ (c & 7)) * 16);
  const int q4 = c >> 2, p4 = c & 3;
  int boff[7];
#pragma unroll
  for (int ni = 0; ni < 7; ++ni) {
    const int pos = slot_pos<KV>(wn, ni);
    boff[ni] = 16384 + (g >> 1) * 7168 + (8 * (g & 1) + q4) * 64 + (p4 & 1) * 8 +
               (((2 * (pos & 1) + (p4 >> 1)) ^ ((g & 1) << 1)) * 16) + (pos >> 1) * 1024;
  }

  f32x4 acc[4][7];
  struct Frags { bf16x8 a[4]; s16x4 bl[7], bh[7]; };
  Frags F0, F1;
  auto rd1 = [&](Frags& F, int buf, int kk, int idx) {
    const char* base = smem + buf * K1_STAGE;
    if (idx < 4) F.a[idx] = *(const bf16x8*)(base + (kk ? aoff1 : aoff0) + idx * 2048);
    else {
      const int ni = (idx - 4) >> 1, hi = (idx - 4) & 1;
      const char* bp = base + boff[ni] + kk * 14336 + hi * 256;
      s16x4 v = __builtin_amdgcn_ds_read_tr16_b64_v4i16((lds_s16x4_ptr)(bp));
      if (hi) F.bh[ni] = v; else F.bl[ni] = v;
    }
  };
  auto rdst = [&](Frags& F, int buf, int kk, int nb, int p0, int np) {
#pragma unroll
    for (int i = 0; i < 6; ++i) {
      rd1(F, buf, kk, 3 * i); rd1(F, buf, kk, 3 * i + 1); rd1(F, buf, kk, 3 * i + 2);
      if (i < np) st1(nb, p0 + i);
    }
  };
  auto mma = [&](Frags& F) {
#pragma unroll
    for (int ni = 0; ni < 7; ++ni) {
      bf16x8 fb = __builtin_shufflevector(F.bl[ni], F.bh[ni], 0, 1, 2, 3, 4, 5, 6, 7);
#pragma unroll
      for (int mi = 0; mi < 4; ++mi) {
        if (KV && ni < 5) acc[mi][ni] = MFMA16(F.a[mi], fb, acc[mi][ni]);
        else acc[mi][ni] = MFMA16(fb, F.a[mi], acc[mi][ni]);
      }
    }
  };

#define K1_INTERLEAVE(NV)                                                   \
  _Pragma("unroll") for (int i_ = 0; i_ < 6; ++i_) {                        \
    __builtin_amdgcn_sched_group_barrier(0x008, 1, 0);                      \
    __builtin_amdgcn_sched_group_barrier(0x100, 1, 0);                      \
    __builtin_amdgcn_sched_group_barrier(0x008, 1, 0);                      \
    __builtin_amdgcn_sched_group_barrier(0x100, 1, 0);                      \
    __builtin_amdgcn_sched_group_barrier(0x008, 1, 0);                      \
    __builtin_amdgcn_sched_group_barrier(0x100, 1, 0);                      \
    if (i_ < (NV)) __builtin_amdgcn_sched_group_barrier(0x010, 1, 0);       \
    __builtin_amdgcn_sched_group_barrier(0x008, 1, 0);                      \
  }                                                                         \
  __builtin_amdgcn_sched_group_barrier(0x008, 4, 0);

  const __amdgpu_buffer_rsrc_t rsO = __builtin_amdgcn_make_buffer_rsrc(ws, 0, (int)OFF_FLAG, 0x00020000);
  if (KV) asm volatile("s_waitcnt vmcnt(11)" ::: "memory");
  else asm volatile("s_waitcnt vmcnt(12)" ::: "memory");
  __builtin_amdgcn_s_barrier();
  asm volatile("" ::: "memory");
  STAMP(0, 1);
#pragma unroll
  for (int ni = 0; ni < 7; ++ni) {
    float iv;
    if (KV) iv = ni < 2 ? 1e-6f : (ni == 2 ? (wn ? 0.f : 1e-6f) : (ni == 5 ? (wn ? 1e-6f : 0.f) : 0.f));
    else iv = ni < 2 ? 1e-6f : 0.f;
#pragma unroll
    for (int mi = 0; mi < 4; ++mi) acc[mi][ni] = (f32x4){iv, iv, iv, iv};
  }
  rdst(F0, 0, 0, 2, 0, 6);
  __builtin_amdgcn_sched_barrier(0);
  rdst(F1, 0, 1, 2, 6, 5);
  mma(F0);
  K1_INTERLEAVE(5)
  __builtin_amdgcn_sched_barrier(0);
  extras();
  __builtin_amdgcn_sched_barrier(0);
  int buf = 1, nbuf = 0;
#pragma unroll 1
  for (int s = 1; s < 8; ++s) {
    if (s < 3) asm volatile("s_waitcnt lgkmcnt(0)\n\ts_waitcnt vmcnt(14)" ::: "memory");
    else asm volatile("s_waitcnt lgkmcnt(0)\n\ts_waitcnt vmcnt(11)" ::: "memory");
    LSTAMP(2 * s);
    __builtin_amdgcn_s_barrier();
    asm volatile("" ::: "memory");
    LSTAMP(2 * s + 1);
    __builtin_amdgcn_sched_barrier(0);
    rdst(F0, buf, 0, nbuf, 0, 6);
    mma(F1);
    K1_INTERLEAVE(6)
    __builtin_amdgcn_sched_barrier(0);
    rdst(F1, buf, 1, nbuf, 6, 5);
    mma(F0);
    K1_INTERLEAVE(5)
    __builtin_amdgcn_sched_barrier(0);
    nbuf = buf;
    buf = buf == 2 ? 0 : buf + 1;
  }
  STAMP(0, 2);
  asm volatile("s_waitcnt lgkmcnt(0)\n\ts_waitcnt vmcnt(11)" ::: "memory");
  __builtin_amdgcn_s_barrier();
  asm volatile("" ::: "memory");
  __builtin_amdgcn_sched_barrier(0);
  {
    const int gwv = min(blockIdx.x * 4 + w, 959);
#pragma unroll
    for (int i = 0; i < 2; ++i) {
      const char* bp = smem + K1_LDS + 6144 + w * 2048 + (4 * g + q4) * 64 + i * 32 + p4 * 8;
      s16x4 lo = __builtin_amdgcn_ds_read_tr16_b64_v4i16((lds_s16x4_ptr)(bp));
      s16x4 hi = __builtin_amdgcn_ds_read_tr16_b64_v4i16((lds_s16x4_ptr)(bp + 1024));
      bf16x8 v = __builtin_shufflevector(lo, hi, 0, 1, 2, 3, 4, 5, 6, 7);
      __builtin_amdgcn_raw_buffer_store_b128(__builtin_bit_cast(u32x4, v), rsO, lane * 16 + (int)OFF_W2F + (gwv * 2 + i) * 1024, 0, 16);
    }
  }
  __builtin_amdgcn_sched_barrier(0);
  rdst(F0, 2, 0, 0, 0, 0);
  mma(F1);
  K1_INTERLEAVE(0)
  __builtin_amdgcn_sched_barrier(0);
  rdst(F1, 2, 1, 0, 0, 0);
  mma(F0);
  K1_INTERLEAVE(0)
  __builtin_amdgcn_sched_barrier(0);
  asm volatile("s_waitcnt lgkmcnt(0)\n\ts_waitcnt vmcnt(2)" ::: "memory");
  __builtin_amdgcn_s_barrier();
  asm volatile("" ::: "memory");
  __builtin_amdgcn_sched_barrier(0);
  rdst(F0, 0, 0, 0, 0, 0);
  mma(F1);
  K1_INTERLEAVE(0)
  __builtin_amdgcn_sched_barrier(0);
  rdst(F1, 0, 1, 0, 0, 0);
  mma(F0);
  K1_INTERLEAVE(0)
  __builtin_amdgcn_sched_barrier(0);
  mma(F1);
  STAMP(0, 3);

  STAMP(0, 6);
  const int rt0 = rb * 8 + wm * 4;
#define K1AUX 16
  const int oHF = (int)OFF_HF + rt0 * 48 * 1024, oQF = (int)OFF_QF + rt0 * 20 * 1024;
  auto st16 = [&](int off, u32x4 v) { __builtin_amdgcn_raw_buffer_store_b128(v, rsO, lane * 16 + off, 0, K1AUX); };
  auto st8 = [&](int off, u32x2 v) { __builtin_amdgcn_raw_buffer_store_b64(v, rsO, lane * 16, off, 0); };
  u32x2 bias[7];
  {
    const char* bl = smem + K1_LDS + 1024 + w * 1024 + g * 8;
#pragma unroll
    for (int ni = 0; ni < 7; ++ni) {
      if (KV) { if (ni >= 5) bias[ni] = *(const u32x2*)(bl + 32 * (ni - 5)); }
      else if (ni >= 2) bias[ni] = *(const u32x2*)(bl + (ni < 6 ? 128 * wn + 32 * (ni - 2) : 256 + 32 * wn));
    }
  }
  auto addb = [&](const f32x4& a, const u32x2& b) -> f32x4 {
    return a + (f32x4){bf_lo(b[0]), bf_hi(b[0]), bf_lo(b[1]), bf_hi(b[1])};
  };

  if (!KV) {
#pragma unroll
    for (int mi = 0; mi < 4; ++mi) {
      u32x2 lo = cvt_q(acc[mi][0]), hi = cvt_q(acc[mi][1]);
      st16(oQF + (mi * 20 + 2 * j + wn) * 1024, (u32x4){lo[0], lo[1], hi[0], hi[1]});
    }
    STAMP(0, 7);
#pragma unroll
    for (int pr = 0; pr < 2; ++pr)
#pragma unroll
      for (int mi = 0; mi < 4; ++mi) {
        u32x2 lo = cvt_h(addb(acc[mi][2 + 2 * pr], bias[2 + 2 * pr])), hi = cvt_h(addb(acc[mi][3 + 2 * pr], bias[3 + 2 * pr]));
        st16(oHF + (mi * 48 + 5 * j + 2 * wn + pr) * 1024, (u32x4){lo[0], lo[1], hi[0], hi[1]});
      }
    STAMP(0, 8); STAMP(0, 9);
#pragma unroll
    for (int mi = 0; mi < 4; ++mi)
      st8(oHF + (mi * 48 + 5 * j + 4) * 1024 + wn * 8, cvt_h(addb(acc[mi][6], bias[6])));
  } else {
    asm volatile("s_waitcnt lgkmcnt(0)" ::: "memory");
    __builtin_amdgcn_s_barrier();
    asm volatile("" ::: "memory");
    STAMP(0, 7);
    char* xk = smem + (wm * 2) * 5 * 1024 + lane * 16;
    char* xv = xk + 20 * 1024;
    if (wn == 0) {
#pragma unroll
      for (int p = 0; p < 2; ++p) {
#pragma unroll
        for (int t = 0; t < 2; ++t) {
          u32x2 lo = cvt_v(acc[2 * p][3 + t]), hi = cvt_v(acc[2 * p + 1][3 + t]);
          *(u32x4*)(xv + (p * 5 + t) * 1024) = (u32x4){lo[0], lo[1], hi[0], hi[1]};
        }
#pragma unroll
        for (int t = 0; t < 3; ++t) {
          u32x2 lo = cvt_q(acc[2 * p][t]), hi = cvt_q(acc[2 * p + 1][t]);
          *(u32x4*)(xk + (p * 5 + t) * 1024) = (u32x4){lo[0], lo[1], hi[0], hi[1]};
        }
      }
    } else {
#pragma unroll
      for (int p = 0; p < 2; ++p) {
#pragma unroll
        for (int t = 0; t < 3; ++t) {
          u32x2 lo = cvt_v(acc[2 * p][2 + t]), hi = cvt_v(acc[2 * p + 1][2 + t]);
          *(u32x4*)(xv + (p * 5 + 2 + t) * 1024) = (u32x4){lo[0], lo[1], hi[0], hi[1]};
        }
#pragma unroll
        for (int t = 0; t < 2; ++t) {
          u32x2 lo = cvt_q(acc[2 * p][t]), hi = cvt_q(acc[2 * p + 1][t]);
          *(u32x4*)(xk + (p * 5 + 3 + t) * 1024) = (u32x4){lo[0], lo[1], hi[0], hi[1]};
        }
      }
    }
    asm volatile("s_waitcnt lgkmcnt(0)" ::: "memory");
    __builtin_amdgcn_s_barrier();
    asm volatile("" ::: "memory");
    STAMP(0, 8);
    const int a0 = w == 0 ? 0 : w - 1;
    bf16x8 pkA[4], vA3[3][4], pkB[4], vB[4];
#pragma unroll
    for (int ks4 = 0; ks4 < 4; ++ks4) {
      pkA[ks4] = *(const bf16x8*)(smem + (ks4 * 5 + w) * 1024 + lane * 16);
#pragma unroll
      for (int t = 0; t < 3; ++t) vA3[t][ks4] = *(const bf16x8*)(smem + (20 + ks4 * 5 + a0 + t) * 1024 + lane * 16);
    }
    const bool hasB = (w == 0 || w == 3);
    const int teB = w == 0 ? 4 : 3;
    if (hasB) {
#pragma unroll
      for (int ks4 = 0; ks4 < 4; ++ks4) {
        pkB[ks4] = *(const bf16x8*)(smem + (ks4 * 5 + 4) * 1024 + lane * 16);
        vB[ks4] = *(const bf16x8*)(smem + (20 + ks4 * 5 + teB) * 1024 + lane * 16);
      }
    }
    if (wn == 0) {
#pragma unroll
      for (int mi = 0; mi < 4; ++mi) {
        u32x2 lo = cvt_h(addb(acc[mi][5], bias[5])), hi = cvt_h(addb(acc[mi][6], bias[6]));
        st16(oHF + (mi * 48 + 40 + j) * 1024, (u32x4){lo[0], lo[1], hi[0], hi[1]});
      }
    } else {
#pragma unroll
      for (int mi = 0; mi < 4; ++mi)
        st8(oQF + (mi * 20 + 16 + (j >> 1)) * 1024 + (j & 1) * 8, cvt_q(acc[mi][5]));
    }
    STAMP(0, 9);
    u16* KVP = (u16*)(ws + OFF_KVP) + (size_t)j * 4 * 6400 + rb * 400;
    u16* ZP = (u16*)(ws + OFF_ZP) + j * 80 * 16 + rb;
    const bf16x8 ones = {0x3f80, 0x3f80, 0x3f80, 0x3f80, 0x3f80, 0x3f80, 0x3f80, 0x3f80};
    auto put_kv = [&](const f32x4& d, int te, int td) {
      const int dg = 16 * td + c, eg = 16 * te + 4 * g;
      const int hd = dg / 20, he = eg / 20;
      if (hd == he) *(u32x2*)(KVP + hd * 6400 + (dg - hd * 20) * 20 + (eg - he * 20)) = cvt_v(d);
    };
    auto put_z = [&](const f32x4& d, int td) {
      if (g == 0) ZP[(16 * td + c) * 16] = (u16)(pack2(d[0], 0.f) & 0xffffu);
    };
    f32x4 dA[4];
#pragma unroll
    for (int t = 0; t < 4; ++t) dA[t] = (f32x4){0.f, 0.f, 0.f, 0.f};
#pragma unroll
    for (int ks4 = 0; ks4 < 4; ++ks4) {
#pragma unroll
      for (int t = 0; t < 3; ++t) dA[t] = MFMA16(vA3[t][ks4], pkA[ks4], dA[t]);
      dA[3] = MFMA16(ones, pkA[ks4], dA[3]);
    }
#pragma unroll
    for (int t = 0; t < 3; ++t) put_kv(dA[t], a0 + t, w);
    put_z(dA[3], w);
    if (hasB) {
      f32x4 dB = {0.f, 0.f, 0.f, 0.f}, dZ = {0.f, 0.f, 0.f, 0.f};
#pragma unroll
      for (int ks4 = 0; ks4 < 4; ++ks4) { dB = MFMA16(vB[ks4], pkB[ks4], dB); dZ = MFMA16(ones, pkB[ks4], dZ); }
      put_kv(dB, teB, 4);
      if (w == 0) put_z(dZ, 4);
    }
  }
}

__global__ __launch_bounds__(256, 2) void k1_kernel(const u16* __restrict__ x, const u16* __restrict__ wqkv,
                                                   const u16* __restrict__ w1, const u16* __restrict__ b1,
                                                   const u16* __restrict__ w2, const u16* __restrict__ gate_w,
                                                   char* __restrict__ ws) {
  extern __shared__ __attribute__((aligned(16))) char smem[];
  const int bid = blockIdx.x;
  const int xcd = bid & 7, idx = bid >> 3;
  const int ri = idx & 7, ci = idx >> 3;
  const int rb = (xcd & 1) * 8 + ri;
  const int cq = (xcd >> 1) * 4 + ci;
  if (bid == 0 && threadIdx.x < 64) ((unsigned*)(ws + OFF_FLAG))[threadIdx.x * 64] = 0u;
  if (idx < 5 && threadIdx.x < 64) {
    const char* pc = (const char*)(__builtin_amdgcn_s_getpc() & ~255ull);
    __builtin_amdgcn_global_load_lds((const unsigned*)(pc + min(idx * 64 + (int)threadIdx.x, 289) * 128), (unsigned*)(smem + K1_LDS), 4, 0, 0);
  }
  if (cq & 1) k1_body<false>(smem, rb, cq >> 1, ci, ri, x, wqkv, w1, b1, w2, gate_w, ws);
  else k1_body<true>(smem, rb, cq >> 1, ci, ri, x, wqkv, w1, b1, w2, gate_w, ws);
  STAMP(0, 4);
  STAMP(0, 5); RSTAMP(0, 31);
}

typedef __attribute__((address_space(1))) unsigned gu32;
#define RLX_AGENT __ATOMIC_RELAXED, __HIP_MEMORY_SCOPE_AGENT
DI void publish(char* ws, int counter, int lane) {
  asm volatile("s_waitcnt vmcnt(0)" ::: "memory");
  if (lane == 0) __hip_atomic_fetch_add((gu32*)(ws + OFF_FLAG) + counter * 64, 1u, RLX_AGENT);
}
DI void task_w(int u, int lane, char* lds  , const u16* __restrict__ wo, char* ws) {
  const __amdgpu_buffer_rsrc_t rs = __builtin_amdgcn_make_buffer_rsrc(ws, 0, (int)OFF_FLAG, 0x00020000);
  const int h = u >> 3, cr = u & 7;
  const int g = lane >> 4, c = lane & 15;
  const u16* KVP = (const u16*)(ws + OFF_KVP) + h * 6400 + min(lane, 49) * 8;
  u32x4 kp[16];
#pragma unroll
  for (int p = 0; p < 16; ++p) kp[p] = *(const u32x4*)(KVP + p * 400);
#pragma unroll
  for (int i = 0; i < 4; ++i) {
    const int q = min(i * 64 + lane, 199), row = q / 10, ch = q - row * 10;
    __builtin_amdgcn_global_load_lds((const unsigned*)(wo + (size_t)(h * 20 + row) * DM + cr * 80 + ch * 8),
                                     (unsigned*)(lds + 2048 + i * 1024), 16, 0, 0);
  }
  __builtin_amdgcn_sched_barrier(0);
  __builtin_amdgcn_s_barrier();
  __builtin_amdgcn_sched_barrier(0);
  STAMP(2, 8);
  f32x4 s0 = {0.f, 0.f, 0.f, 0.f}, s1 = {0.f, 0.f, 0.f, 0.f};
#pragma unroll
  for (int p = 0; p < 16; ++p) {
    s0 += (f32x4){bf_lo(kp[p][0]), bf_hi(kp[p][0]), bf_lo(kp[p][1]), bf_hi(kp[p][1])};
    s1 += (f32x4){bf_lo(kp[p][2]), bf_hi(kp[p][2]), bf_lo(kp[p][3]), bf_hi(kp[p][3])};
  }
  asm volatile("s_waitcnt vmcnt(0)" ::: "memory");
  STAMP(2, 9);
  bf16x8 wf[5];
  {
    const int r0 = min(8 * g + (c >> 2), 19), r1 = min(8 * g + 4 + (c >> 2), 19);
#pragma unroll
    for (int t = 0; t < 5; ++t) {
      s16x4 lo = __builtin_amdgcn_ds_read_tr16_b64_v4i16((lds_s16x4_ptr)(lds + 2048 + r0 * 160 + t * 32 + (c & 3) * 8));
      s16x4 hi = __builtin_amdgcn_ds_read_tr16_b64_v4i16((lds_s16x4_ptr)(lds + 2048 + r1 * 160 + t * 32 + (c & 3) * 8));
      wf[t] = __builtin_shufflevector(lo, hi, 0, 1, 2, 3, 4, 5, 6, 7);
    }
  }
  *(u32x4*)(lds + lane * 32) = (u32x4){0u, 0u, 0u, 0u};
  *(u32x4*)(lds + lane * 32 + 16) = (u32x4){0u, 0u, 0u, 0u};
  __builtin_amdgcn_s_waitcnt(0xC07F);
  if (lane < 50) {
    const int i0 = lane * 8, d0 = i0 / 20, e0 = i0 - d0 * 20;
    *(u32x2*)(lds + d0 * 64 + e0 * 2) = (u32x2){pack2(s0[0], s0[1]), pack2(s0[2], s0[3])};
    const int i1 = i0 + 4, d1 = i1 / 20, e1 = i1 - d1 * 20;
    *(u32x2*)(lds + d1 * 64 + e1 * 2) = (u32x2){pack2(s1[0], s1[1]), pack2(s1[2], s1[3])};
  }
  __builtin_amdgcn_s_waitcnt(0xC07F);
  bf16x8 ka0 = *(const bf16x8*)(lds + c * 64 + g * 16);
  bf16x8 ka1 = *(const bf16x8*)(lds + (16 + c) * 64 + g * 16);
#pragma unroll
  for (int t = 0; t < 5; ++t) {
    f32x4 d0 = MFMA16(ka0, wf[t], ((f32x4){0.f, 0.f, 0.f, 0.f}));
    f32x4 d1 = MFMA16(ka1, wf[t], ((f32x4){0.f, 0.f, 0.f, 0.f}));
    {
      const int k = h * 20 + 4 * g;
      const int kb = k >> 5, half = (k >> 4) & 1, gg = (k & 15) >> 2;
      __builtin_amdgcn_raw_buffer_store_b64((u32x2){pack2(d0[0], d0[1]), pack2(d0[2], d0[3])}, rs,
          (int)OFF_WPF + (kb * 40 + cr * 5 + t) * 1024 + half * 512 + (gg * 16 + c) * 8, 0, 16);
    }
    if (g == 0) {
      const int k = h * 20 + 16;
      const int kb = k >> 5, half = (k >> 4) & 1, gg = (k & 15) >> 2;
      __builtin_amdgcn_raw_buffer_store_b64((u32x2){pack2(d1[0], d1[1]), pack2(d1[2], d1[3])}, rs,
          (int)OFF_WPF + (kb * 40 + cr * 5 + t) * 1024 + half * 512 + (gg * 16 + c) * 8, 0, 16);
    }
  }
  STAMP(2, 10);
  publish(ws, 32 + cr, lane);
}

DI void task_a(int u, int lane, char* lds  , char* ws) {
  const __amdgpu_buffer_rsrc_t rs = __builtin_amdgcn_make_buffer_rsrc(ws, 0, (int)OFF_FLAG, 0x00020000);
  const int rt = u >> 1, kh = u & 1;
  const int g = lane >> 4, c = lane & 15;
  const u16* ZP = (const u16*)(ws + OFF_ZP) + kh * 320 * 16;
  u32x4 zp[10];
#pragma unroll
  for (int i = 0; i < 10; ++i) zp[i] = *(const u32x4*)(ZP + i * 512 + lane * 8);
  const char* QF = ws + OFF_QF + ((size_t)rt * 20 + kh * 10) * 1024;
  u32x4 qf[10];
#pragma unroll
  for (int b = 0; b < 10; ++b) qf[b] = *(const u32x4*)(QF + b * 1024 + lane * 16);
  const float gate = ((const float*)(ws + OFF_G))[rt * 16 + c];
  __builtin_amdgcn_sched_barrier(0);
  __builtin_amdgcn_s_barrier();
  __builtin_amdgcn_sched_barrier(0);
  STAMP(2, 8);
#pragma unroll
  for (int i = 0; i < 10; ++i) {
    float t = ((bf_lo(zp[i][0]) + bf_hi(zp[i][0])) + (bf_lo(zp[i][1]) + bf_hi(zp[i][1]))) +
              ((bf_lo(zp[i][2]) + bf_hi(zp[i][2])) + (bf_lo(zp[i][3]) + bf_hi(zp[i][3])));
    t += __builtin_bit_cast(float, __builtin_amdgcn_update_dpp(0, __builtin_bit_cast(int, t), 0xB1, 0xF, 0xF, true));
    if ((lane & 1) == 0) *(u16*)(lds + (i * 32 + (lane >> 1)) * 2) = (u16)(pack2(t, 0.f) & 0xffffu);
  }
  STAMP(2, 9);
  __builtin_amdgcn_s_waitcnt(0xC07F);
  f32x4 den = {0.f, 0.f, 0.f, 0.f};
#pragma unroll
  for (int b = 0; b < 10; ++b) {
    const int k0 = 32 * b + 4 * g, k1 = k0 + 16;
    u32x2 zl = *(const u32x2*)(lds + k0 * 2), zh = *(const u32x2*)(lds + k1 * 2);
    const bool m0 = (k0 / 20) == c, m1 = (k1 / 20) == c;
    u32x4 zf = {m0 ? zl[0] : 0u, m0 ? zl[1] : 0u, m1 ? zh[0] : 0u, m1 ? zh[1] : 0u};
    den = MFMA16(__builtin_bit_cast(bf16x8, zf), __builtin_bit_cast(bf16x8, qf[b]), den);
  }
  f32x4 rd;
#pragma unroll
  for (int r = 0; r < 4; ++r) rd[r] = gate / (den[r] + 1e-6f);
  *(f32x4*)(lds + 1024 + c * 64 + g * 16) = rd;
  __builtin_amdgcn_s_waitcnt(0xC07F);
#pragma unroll
  for (int b = 0; b < 10; ++b) {
    const int k0 = 32 * b + 4 * g, k1 = k0 + 16;
    const float r0 = *(const float*)(lds + 1024 + c * 64 + (k0 / 20) * 4);
    const float r1 = *(const float*)(lds + 1024 + c * 64 + (k1 / 20) * 4);
    u32x4 o;
    o[0] = pack2(bf_lo(qf[b][0]) * r0, bf_hi(qf[b][0]) * r0);
    o[1] = pack2(bf_lo(qf[b][1]) * r0, bf_hi(qf[b][1]) * r0);
    o[2] = pack2(bf_lo(qf[b][2]) * r1, bf_hi(qf[b][2]) * r1);
    o[3] = pack2(bf_lo(qf[b][3]) * r1, bf_hi(qf[b][3]) * r1);
    __builtin_amdgcn_raw_buffer_store_b128(o, rs, (int)OFF_AF + (rt * 20 + kh * 10 + b) * 1024 + lane * 16, 0, 16);
  }
  STAMP(2, 10);
  publish(ws, rt >> 2, lane);
}

constexpr int K2_DEPTH = 3;
__global__ __launch_bounds__(384, 2) void k2_kernel(const u16* __restrict__ wo, const u16* __restrict__ b2,
                                                   char* ws, u16* __restrict__ out) {
  __shared__ __attribute__((aligned(16))) char red[4 * 20 * 1024 + 8192 + 16];
  typedef __attribute__((address_space(3))) unsigned lu32;
  lu32* ready = (lu32*)(red + 81920 + 8192);
  const int bid = blockIdx.x;
  const int xcd = bid & 7, idx = bid >> 3;
  const int rb = (xcd >> 1) * 8 + (idx & 7);
  const int cb = (xcd & 1) * 4 + (idx >> 3);
  const int lane = threadIdx.x & 63;
  const int w = __builtin_amdgcn_readfirstlane(threadIdx.x >> 6);
  const int g = lane >> 4, c = lane & 15;

  if (w >= 4) {
    __builtin_amdgcn_s_setprio(3);
    RSTAMP(2, 30); STAMP(2, 0);
    if (w == 4) {
      if (lane < 2) __hip_atomic_store(ready + lane, 0u, __ATOMIC_RELAXED, __HIP_MEMORY_SCOPE_WORKGROUP);
      asm volatile("s_waitcnt lgkmcnt(0)" ::: "memory");
      task_w(bid, lane, red + 81920, wo, ws);
      STAMP(2, 1);
    } else {
      task_a(bid, lane, red + 81920 + 6144, ws);
      STAMP(2, 1);
      gu32* cntA = (gu32*)(ws + OFF_FLAG) + rb * 64;
      gu32* cntW = (gu32*)(ws + OFF_FLAG) + (32 + cb) * 64;
      for (unsigned spins = 0; spins < (1u << 24); ++spins) {
        if (__builtin_amdgcn_readfirstlane(__hip_atomic_load(ready + 1, __ATOMIC_RELAXED, __HIP_MEMORY_SCOPE_WORKGROUP)) != 0u) break;
        __builtin_amdgcn_s_sleep(2);
      }
      for (unsigned spins = 0; spins < (1u << 22); ++spins) {
        const unsigned a = __hip_atomic_load(cntA, RLX_AGENT), b = __hip_atomic_load(cntW, RLX_AGENT);
        if (__builtin_amdgcn_readfirstlane(a) == 8u && __builtin_amdgcn_readfirstlane(b) == 32u) break;
        __builtin_amdgcn_s_sleep(4);
      }
      if (lane == 0) __hip_atomic_store(ready, 1u, __ATOMIC_RELAXED, __HIP_MEMORY_SCOPE_WORKGROUP);
    }
    STAMP(2, 2); STAMP(2, 3); STAMP(2, 4); STAMP(2, 5); RSTAMP(2, 31);
    return;
  }

  const __amdgpu_buffer_rsrc_t rs = __builtin_amdgcn_make_buffer_rsrc(ws, 0, (int)OFF_FLAG, 0x00020000);
  const int voff = lane * 16;
  const int oH = (int)OFF_HF + (rb * 4 * 48 + w) * 1024;
  const int oA = (int)OFF_AF + (rb * 4 * 20 + w) * 1024;
  const int oW2 = (int)OFF_W2F + (w * 40 + cb * 5) * 1024;
  const int oWP = (int)OFF_WPF + (w * 40 + cb * 5) * 1024;

  RSTAMP(2, 30); STAMP(2, 0);
  f32x4 acc[4][5];
#pragma unroll
  for (int mi = 0; mi < 4; ++mi)
#pragma unroll
    for (int ni = 0; ni < 5; ++ni) acc[mi][ni] = (f32x4){0.f, 0.f, 0.f, 0.f};

  constexpr int NS = K2_DEPTH + 1;
  u32x4 fa[NS][4], fb[NS][5];
  float gv[4];
  u32x2 b2v[5];
#define K2ROTR 0
#define K2ROTC 0
  const int rot = (((idx & 7) * K2ROTR) / 2 + ((idx >> 3) * K2ROTC) / 2) % 12;
  auto load = [&](int i, int slot) {
    if (i < 12) {
      int ir = i + rot; ir = ir >= 12 ? ir - 12 : ir;
#pragma unroll
      for (int ni = 0; ni < 5; ++ni) fb[slot][ni] = __builtin_amdgcn_raw_buffer_load_b128(rs, voff, oW2 + (4 * ir * 40 + ni) * 1024, 0);
#pragma unroll
      for (int mi = 0; mi < 4; ++mi) fa[slot][mi] = __builtin_amdgcn_raw_buffer_load_b128(rs, voff, oH + (mi * 48 + 4 * ir) * 1024, 0);
    } else {
      const int ii = i - 12;
#pragma unroll
      for (int ni = 0; ni < 5; ++ni) {
        u32x2 lo = __builtin_amdgcn_raw_buffer_load_b64(rs, lane * 8, oWP + (4 * ii * 40 + ni) * 1024, 16);
        u32x2 hi = __builtin_amdgcn_raw_buffer_load_b64(rs, lane * 8, oWP + (4 * ii * 40 + ni) * 1024 + 512, 16);
        fb[slot][ni] = (u32x4){lo[0], lo[1], hi[0], hi[1]};
      }
#pragma unroll
      for (int mi = 0; mi < 4; ++mi) fa[slot][mi] = __builtin_amdgcn_raw_buffer_load_b128(rs, voff, oA + (mi * 20 + 4 * ii) * 1024, 16);
    }
  };
  __builtin_amdgcn_s_barrier();
  __builtin_amdgcn_sched_barrier(0);
#pragma unroll
  for (int d = 0; d < K2_DEPTH; ++d) load(d, d);
#pragma unroll
  for (int mi = 0; mi < 4; ++mi)
    gv[mi] = __uint_as_float(__builtin_amdgcn_raw_buffer_load_b32(rs, c * 4, (int)OFF_G + (rb * 64 + mi * 16) * 4, 0));
  __builtin_amdgcn_sched_barrier(0);
#pragma unroll
  for (int i = 0; i < 17; ++i) {
    const int slot = i % NS;
    if (i == 1) { STAMP(2, 1); }
#define K2GO 6
    if (i == K2GO) {
      if (lane == 0) __hip_atomic_store(ready + 1, 1u, __ATOMIC_RELAXED, __HIP_MEMORY_SCOPE_WORKGROUP);
      __builtin_amdgcn_sched_barrier(0);
    }
    if (i == 12 - K2_DEPTH) {
      STAMP(2, 6);
      for (unsigned spins = 0; spins < (1u << 24); ++spins) {
        if (__builtin_amdgcn_readfirstlane(__hip_atomic_load(ready, __ATOMIC_RELAXED, __HIP_MEMORY_SCOPE_WORKGROUP)) != 0u) break;
        __builtin_amdgcn_s_sleep(1);
      }
      asm volatile("" ::: "memory");
      STAMP(2, 7);
      __builtin_amdgcn_sched_barrier(0);
    }
    if (i == 12) {
      STAMP(2, 2);
#pragma unroll
      for (int mi = 0; mi < 4; ++mi) {
        gv[mi] = 1.0f - gv[mi];
#pragma unroll
        for (int ni = 0; ni < 5; ++ni) acc[mi][ni] *= gv[mi];
      }
      __builtin_amdgcn_sched_barrier(0);
    }
    if (i + K2_DEPTH < 17) load(i + K2_DEPTH, (i + K2_DEPTH) % NS);
    if (i == 15) {
#pragma unroll
      for (int ni = 0; ni < 5; ++ni) b2v[ni] = *(const u32x2*)(b2 + cb * 80 + ni * 16 + 4 * g);
      __builtin_amdgcn_sched_barrier(0);
    }
#pragma unroll
    for (int mi = 0; mi < 4; ++mi)
#pragma unroll
      for (int ni = 0; ni < 5; ++ni)
        acc[mi][ni] = MFMA16(__builtin_bit_cast(bf16x8, fb[slot][ni]), __builtin_bit_cast(bf16x8, fa[slot][mi]), acc[mi][ni]);
    if (i + K2_DEPTH < 12) {
#pragma unroll
      for (int k = 0; k < 9; ++k) {
        __builtin_amdgcn_sched_group_barrier(0x008, 2, 0);
        __builtin_amdgcn_sched_group_barrier(0x020, 1, 0);
      }
      __builtin_amdgcn_sched_group_barrier(0x008, 2, 0);
    } else if (i + K2_DEPTH < 17) {
#pragma unroll
      for (int k = 0; k < 14; ++k) {
        __builtin_amdgcn_sched_group_barrier(0x008, 1, 0);
        __builtin_amdgcn_sched_group_barrier(0x020, 1, 0);
      }
      __builtin_amdgcn_sched_group_barrier(0x008, 6, 0);
    }
    __builtin_amdgcn_sched_barrier(0);
  }

  STAMP(2, 3);
#pragma unroll
  for (int mi = 0; mi < 4; ++mi)
    if (mi != w) {
#pragma unroll
      for (int ni = 0; ni < 5; ++ni) *(f32x4*)(red + ((w * 4 + mi) * 5 + ni) * 1024 + lane * 16) = acc[mi][ni];
    }
  __syncthreads();
  float gw = gv[0];
  f32x4 mine[5];
#pragma unroll
  for (int ni = 0; ni < 5; ++ni) mine[ni] = acc[0][ni];
#pragma unroll
  for (int mi = 1; mi < 4; ++mi)
    if (w == mi) {
      gw = gv[mi];
#pragma unroll
      for (int ni = 0; ni < 5; ++ni) mine[ni] = acc[mi][ni];
    }
#pragma unroll
  for (int ni = 0; ni < 5; ++ni) {
    f32x4 s = mine[ni];
#pragma unroll
    for (int o = 1; o < 4; ++o) {
      const int ow = (w + o) & 3;
      s += *(const f32x4*)(red + ((ow * 4 + w) * 5 + ni) * 1024 + lane * 16);
    }
    const int n = cb * 80 + ni * 16 + 4 * g;
    const u32x2 bv = b2v[ni];
    s[0] += gw * bf_lo(bv[0]); s[1] += gw * bf_hi(bv[0]); s[2] += gw * bf_lo(bv[1]); s[3] += gw * bf_hi(bv[1]);
    *(u32x2*)(out + (size_t)(rb * 64 + w * 16 + c) * DM + n) = (u32x2){pack2(s[0], s[1]), pack2(s[2], s[3])};
  }
  STAMP(2, 4);
  STAMP(2, 5); RSTAMP(2, 31);
}

extern "C" void kernel_launch(void* const* d_in, const int* in_sizes, int n_in, void* d_out, int out_size,
                              void* d_ws, size_t ws_size, hipStream_t stream) {
  const u16* x = (const u16*)d_in[0];
  const u16* wqkv = (const u16*)d_in[1];
  const u16* wo = (const u16*)d_in[2];
  const u16* w1 = (const u16*)d_in[4];
  const u16* b1 = (const u16*)d_in[5];
  const u16* w2 = (const u16*)d_in[6];
  const u16* b2 = (const u16*)d_in[7];
  const u16* gw = (const u16*)d_in[8];
  char* ws = (char*)d_ws;
  (void)hipFuncSetAttribute((const void*)k1_kernel, hipFuncAttributeMaxDynamicSharedMemorySize, K1_LDS + 14336);
  k1_kernel<<<256, 256, K1_LDS + 14336, stream>>>(x, wqkv, w1, b1, w2, gw, ws);
  k2_kernel<<<256, 384, 0, stream>>>(wo, b2, ws, (u16*)d_out);
}
```

```cpp
#include <hip/hip_runtime.h>
#include <hip/hip_bf16.h>

typedef unsigned short u16;
typedef short bf16x8 __attribute__((ext_vector_type(8)));
typedef short s16x4 __attribute__((ext_vector_type(4)));
typedef float f32x4 __attribute__((ext_vector_type(4)));
typedef unsigned u32x4 __attribute__((ext_vector_type(4)));
typedef unsigned u32x2 __attribute__((ext_vector_type(2)));
typedef __attribute__((address_space(3))) s16x4* lds_s16x4_ptr;
#define DI __device__ __forceinline__
#define MFMA16(a, b, c) __builtin_amdgcn_mfma_f32_16x16x32_bf16((a), (b), (c), 0, 0, 0)

constexpr int SEQ = 2048, DM = 640, FF = 1536, NQKV = 1920;

constexpr size_t OFF_HF = 0;
constexpr size_t OFF_QF = OFF_HF + 128ull * 48 * 1024;
constexpr size_t OFF_AF = OFF_QF + 128ull * 20 * 1024;
constexpr size_t OFF_W2F = OFF_AF + 128ull * 20 * 1024;
constexpr size_t OFF_WPF = OFF_W2F + 48ull * 40 * 1024;
constexpr size_t OFF_KVP = OFF_WPF + 20ull * 40 * 1024;
constexpr size_t OFF_ZP = OFF_KVP + 16ull * 32 * 1600;
constexpr size_t OFF_G = OFF_ZP + 16ull * 640 * 4;
constexpr size_t OFF_FLAG = OFF_G + 2048 * 4;
constexpr size_t OFF_DBG = 64ull << 20;
#define STAMP(kern, idx)
#define RSTAMP(kern, idx)
#define LSTAMP(idx)

DI unsigned pack2(float lo, float hi) {
  typedef __bf16 bf2 __attribute__((ext_vector_type(2)));
  typedef float f2 __attribute__((ext_vector_type(2)));
  f2 v = {lo, hi};
  bf2 r = __builtin_convertvector(v, bf2);
  return __builtin_bit_cast(unsigned, r);
}
DI float bf_lo(unsigned u) { return __uint_as_float(u << 16); }
DI float bf_hi(unsigned u) { return __uint_as_float(u & 0xffff0000u); }
DI float rbf(float f) { return bf_lo(pack2(f, 0.f) & 0xffffu); }
DI float phi(float t) {
  float e = __builtin_amdgcn_exp2f(t * 1.4426950408889634f);
  e = fminf(e, 1.0f);
  return fmaxf(t, 0.f) + e + 1e-6f;
}

constexpr int K1_STAGE = 45056;
constexpr int K1_LDS = 3 * K1_STAGE;

struct ColSrc { const u16* p; int stride; };
template <bool KV> DI ColSrc pos_src(int pos, int j, const u16* wqkv, const u16* w1) {
  ColSrc s;
  if (KV) {
    if (pos < 5) { s.p = wqkv + 640 + 80 * j + 16 * pos; s.stride = NQKV; }
    else if (pos == 5) { s.p = wqkv + 16 * (32 + j); s.stride = NQKV; }
    else if (pos < 11) { s.p = wqkv + 1280 + 80 * j + 16 * (pos - 6); s.stride = NQKV; }
    else if (pos == 11) { s.p = wqkv + 1280 + 80 * j + 64; s.stride = NQKV; }
    else { s.p = w1 + 32 * (40 + j) + 16 * (pos - 12); s.stride = FF; }
  } else {
    if (pos < 2) { s.p = wqkv + 32 * (2 * j) + 16 * pos; s.stride = NQKV; }
    else if (pos < 6) { s.p = w1 + 32 * (5 * j) + 16 * (pos - 2); s.stride = FF; }
    else if (pos < 8) { s.p = wqkv + 32 * (2 * j + 1) + 16 * (pos - 6); s.stride = NQKV; }
    else { s.p = w1 + 32 * (5 * j + 2) + 16 * (pos - 8); s.stride = FF; }
  }
  return s;
}
template <bool KV> DI int slot_pos(int wn, int ni) {
  if (KV) {
    const int p0 = ni < 3 ? ni : (ni < 5 ? ni + 3 : ni + 7);
    const int p1 = ni < 2 ? ni + 3 : (ni < 5 ? ni + 6 : (ni == 5 ? 5 : 11));
    return wn ? p1 : p0;
  }
  return ni < 6 ? 6 * wn + ni : 12 + wn;
}
DI float relu_f(float v) { return __int_as_float(max(__float_as_int(v), 0)); }
DI float phi1(float t1) {
  float e = __builtin_amdgcn_exp2f(__builtin_fmaf(t1, 1.4426950408889634f, -1.4426950408889634e-6f));
  e = __builtin_amdgcn_fmed3f(e, 0.0f, 1.0f);
  return __int_as_float(max(__float_as_int(t1), __float_as_int(1e-6f))) + e;
}
DI u32x2 cvt_q(const f32x4& a) { return (u32x2){pack2(phi1(a[0]), phi1(a[1])), pack2(phi1(a[2]), phi1(a[3]))}; }
DI u32x2 cvt_h(const f32x4& a) {
  float t0 = relu_f(a[0]), t1 = relu_f(a[1]), t2 = relu_f(a[2]), t3 = relu_f(a[3]);
  return (u32x2){pack2(t0 * t0, t1 * t1), pack2(t2 * t2, t3 * t3)};
}
DI u32x2 cvt_v(const f32x4& a) { return (u32x2){pack2(a[0], a[1]), pack2(a[2], a[3])}; }

template <bool KV>
DI void k1_body(char* smem, int rb, int j, int ci, int ri, const u16* __restrict__ x, const u16* __restrict__ wqkv,
                const u16* __restrict__ w1, const u16* __restrict__ b1, const u16* __restrict__ w2,
                const u16* __restrict__ gate_w, char* __restrict__ ws) {
  const int tid = threadIdx.x;
  const int lane = tid & 63;
  const int w = __builtin_amdgcn_readfirstlane(tid >> 6);
  const int wm = w >> 1, wn = w & 1;
  const int g = lane >> 4, c = lane & 15;
  RSTAMP(0, 30); STAMP(0, 0);

  const __amdgpu_buffer_rsrc_t rsX = __builtin_amdgcn_make_buffer_rsrc((void*)x, 0, SEQ * DM * 2, 0x00020000);
  const __amdgpu_buffer_rsrc_t rsQ = __builtin_amdgcn_make_buffer_rsrc((void*)wqkv, 0, DM * NQKV * 2, 0x00020000);
  const __amdgpu_buffer_rsrc_t rsW = __builtin_amdgcn_make_buffer_rsrc((void*)w1, 0, DM * FF * 2, 0x00020000);
  const int vA = (lane >> 3) * (DM * 2) + (((lane & 7) ^ ((lane >> 3) & 7)) * 16);
#define NCOM 2
  const int rot = NCOM ? (ri + 2 * ci) % (10 - NCOM) : (ri + (5 * ci) / 2) % 10;
  int ks = NCOM ? 0 : rot, cnt = 0;
  int sA = (rb * 128 + 32 * w) * (DM * 2) + ks * 128;
  const int r16 = lane >> 2;
  const int cs = (lane & 3) ^ (((r16 >> 3) & 1) << 1);
  const int vQ = r16 * (NQKV * 2) + cs * 16;
  const int vW = r16 * (FF * 2) + cs * 16;
  const int vS = r16 * (NQKV * 2) + (cs & 1) * 16 + ((cs >> 1) ? 0 : ((640 + 80 * j + 64) - 16 * (32 + j)) * 2);
  const int vU = r16 * (NQKV * 2) + (cs & 1) * 16;
  int sQ = (w * 16 + ks * 64) * (NQKV * 2), sW = (w * 16 + ks * 64) * (FF * 2);
  auto st1 = [&](int buf, int i) {
    char* base = smem + buf * K1_STAGE;
    typedef __attribute__((address_space(3))) void* ldsp;
    if (i < 4) {
      __builtin_amdgcn_raw_ptr_buffer_load_lds(rsX, (ldsp)(base + (w * 4 + i) * 1024), 16, vA, sA + i * 8 * DM * 2, 0, 0);

    } else {
      const int p = i - 4;
      ldsp dst = (ldsp)(base + 16384 + (w * 7 + p) * 1024);
      const int col = (int)(pos_src<KV>((KV && p == 2) ? 5 : 2 * p, j, (const u16*)0, (const u16*)0).p - (const u16*)0) * 2;
      const bool isW = KV ? (p == 6) : !(p == 0 || p == 3);
      if (isW) __builtin_amdgcn_raw_ptr_buffer_load_lds(rsW, dst, 16, vW, sW + col, 0, 0);
      else __builtin_amdgcn_raw_ptr_buffer_load_lds(rsQ, dst, 16, (KV && p == 2) ? vS : ((KV && p == 5) ? vU : vQ), sQ + col, 0, 0);
      if (p == 6) {
        ++cnt;
        const int adv = cnt == NCOM ? 1 + rot : (ks == 9 ? -(9 - NCOM) : 1);
        ks += adv; sA += adv * 128; sQ += adv * (64 * NQKV * 2); sW += adv * (64 * FF * 2);
      }
    }
  };
  asm volatile("" ::: "memory");
  const int grow = rb * 128 + j * 16 + w * 4 + g;
  u32x4 gx[5], gwv[5];
  if (!KV) {
    const u16* xp = x + (size_t)grow * DM + c * 8;
    const u16* gp = gate_w + c * 8;
#pragma unroll
    for (int i = 0; i < 5; ++i) {
      asm volatile("global_load_dwordx4 %0, %1, off offset:%2" : "=&v"(gx[i]) : "v"(xp), "n"(i * 256) : "memory");
      asm volatile("global_load_dwordx4 %0, %1, off offset:%2" : "=&v"(gwv[i]) : "v"(gp), "n"(i * 256) : "memory");
    }
  }
#pragma unroll
  for (int i = 0; i < 11; ++i) st1(0, i);
#pragma unroll
  for (int i = 0; i < 11; ++i) st1(1, i);
  if (!KV) {
    asm volatile("s_waitcnt vmcnt(22)"
                 : "+v"(gx[0]), "+v"(gx[1]), "+v"(gx[2]), "+v"(gx[3]), "+v"(gx[4]),
                   "+v"(gwv[0]), "+v"(gwv[1]), "+v"(gwv[2]), "+v"(gwv[3]), "+v"(gwv[4]) :: "memory");
    typedef __bf16 bf2 __attribute__((ext_vector_type(2)));
    float t0 = 0.f, t1 = 0.f;
#pragma unroll
    for (int i = 0; i < 5; ++i)
#pragma unroll
      for (int e = 0; e < 4; e += 2) {
        const unsigned xa = gx[i][e], wa = gwv[i][e], xb = gx[i][e + 1], wb = gwv[i][e + 1];
        t0 = __builtin_amdgcn_fdot2_f32_bf16(__builtin_bit_cast(bf2, xa), __builtin_bit_cast(bf2, wa), t0, false);
        t1 = __builtin_amdgcn_fdot2_f32_bf16(__builtin_bit_cast(bf2, xb), __builtin_bit_cast(bf2, wb), t1, false);
      }
    float t = t0 + t1;
    t += __shfl_xor(t, 1); t += __shfl_xor(t, 2); t += __shfl_xor(t, 4); t += __shfl_xor(t, 8);
    ((float*)(ws + OFF_G))[grow] = 1.0f / (1.0f + __expf(-t));
  }
  auto extras = [&]() {
    const u16* bsrc = b1 + (KV ? 32 * (40 + j) : 160 * j) + min(lane, KV ? 3 : 19) * 8;
    __builtin_amdgcn_global_load_lds((const unsigned*)bsrc, (unsigned*)(smem + K1_LDS + 1024 + w * 1024), 16, 0, 0);
    const int gwv = min(blockIdx.x * 4 + w, 959), kb = gwv / 20, np = gwv % 20;
#pragma unroll
    for (int i = 0; i < 2; ++i)
      __builtin_amdgcn_global_load_lds((const unsigned*)(w2 + (size_t)(kb * 32 + i * 16 + (lane >> 2)) * DM + np * 32 + (lane & 3) * 8),
                                       (unsigned*)(smem + K1_LDS + 6144 + (w * 2 + i) * 1024), 16, 0, 0);
  };
  asm volatile("" ::: "memory");

  const int aoff0 = (wm * 64 + c) * 128 + ((g ^ (c & 7)) * 16);
  const int aoff1 = (wm * 64 + c) * 128 + (((g + 4) ^ (c & 7)) * 16);
  const int q4 = c >> 2, p4 = c & 3;
  int boff[7];
#pragma unroll
  for (int ni = 0; ni < 7; ++ni) {
    const int pos = slot_pos<KV>(wn, ni);
    boff[ni] = 16384 + (g >> 1) * 7168 + (8 * (g & 1) + q4) * 64 + (p4 & 1) * 8 +
               (((2 * (pos & 1) + (p4 >> 1)) ^ ((g & 1) << 1)) * 16) + (pos >> 1) * 1024;
  }

  f32x4 acc[4][7];
  struct Frags { bf16x8 a[4]; s16x4 bl[7], bh[7]; };
  Frags F0, F1;
  auto rd1 = [&](Frags& F, int buf, int kk, int idx) {
    const char* base = smem + buf * K1_STAGE;
    if (idx < 4) F.a[idx] = *(const bf16x8*)(base + (kk ? aoff1 : aoff0) + idx * 2048);
    else {
      const int ni = (idx - 4) >> 1, hi = (idx - 4) & 1;
      const char* bp = base + boff[ni] + kk * 14336 + hi * 256;
      s16x4 v = __builtin_amdgcn_ds_read_tr16_b64_v4i16((lds_s16x4_ptr)(bp));
      if (hi) F.bh[ni] = v; else F.bl[ni] = v;
    }
  };
  auto rdst = [&](Frags& F, int buf, int kk, int nb, int p0, int np) {
#pragma unroll
    for (int i = 0; i < 6; ++i) {
      rd1(F, buf, kk, 3 * i); rd1(F, buf, kk, 3 * i + 1); rd1(F, buf, kk, 3 * i + 2);
      if (i < np) st1(nb, p0 + i);
    }
  };
  auto mma = [&](Frags& F) {
#pragma unroll
    for (int ni = 0; ni < 7; ++ni) {
      bf16x8 fb = __builtin_shufflevector(F.bl[ni], F.bh[ni], 0, 1, 2, 3, 4, 5, 6, 7);
#pragma unroll
      for (int mi = 0; mi < 4; ++mi) {
        if (KV && ni < 5) acc[mi][ni] = MFMA16(F.a[mi], fb, acc[mi][ni]);
        else acc[mi][ni] = MFMA16(fb, F.a[mi], acc[mi][ni]);
      }
    }
  };

#define K1_INTERLEAVE(NV)                                                   \
  _Pragma("unroll") for (int i_ = 0; i_ < 6; ++i_) {                        \
    __builtin_amdgcn_sched_group_barrier(0x008, 1, 0);                      \
    __builtin_amdgcn_sched_group_barrier(0x100, 1, 0);                      \
    __builtin_amdgcn_sched_group_barrier(0x008, 1, 0);                      \
    __builtin_amdgcn_sched_group_barrier(0x100, 1, 0);                      \
    __builtin_amdgcn_sched_group_barrier(0x008, 1, 0);                      \
    __builtin_amdgcn_sched_group_barrier(0x100, 1, 0);                      \
    if (i_ < (NV)) __builtin_amdgcn_sched_group_barrier(0x010, 1, 0);       \
    __builtin_amdgcn_sched_group_barrier(0x008, 1, 0);                      \
  }                                                                         \
  __builtin_amdgcn_sched_group_barrier(0x008, 4, 0);

  const __amdgpu_buffer_rsrc_t rsO = __builtin_amdgcn_make_buffer_rsrc(ws, 0, (int)OFF_FLAG, 0x00020000);
  if (KV) asm volatile("s_waitcnt vmcnt(11)" ::: "memory");
  else asm volatile("s_waitcnt vmcnt(12)" ::: "memory");
  __builtin_amdgcn_s_barrier();
  asm volatile("" ::: "memory");
  STAMP(0, 1);
#pragma unroll
  for (int ni = 0; ni < 7; ++ni) {
    float iv;
    if (KV) iv = ni < 2 ? 1e-6f : (ni == 2 ? (wn ? 0.f : 1e-6f) : (ni == 5 ? (wn ? 1e-6f : 0.f) : 0.f));
    else iv = ni < 2 ? 1e-6f : 0.f;
#pragma unroll
    for (int mi = 0; mi < 4; ++mi) acc[mi][ni] = (f32x4){iv, iv, iv, iv};
  }
  rdst(F0, 0, 0, 2, 0, 6);
  __builtin_amdgcn_sched_barrier(0);
  rdst(F1, 0, 1, 2, 6, 5);
  mma(F0);
  K1_INTERLEAVE(5)
  __builtin_amdgcn_sched_barrier(0);
  extras();
  __builtin_amdgcn_sched_barrier(0);
  int buf = 1, nbuf = 0;
#pragma unroll 1
  for (int s = 1; s < 8; ++s) {
    if (s < 3) asm volatile("s_waitcnt lgkmcnt(0)\n\ts_waitcnt vmcnt(14)" ::: "memory");
    else asm volatile("s_waitcnt lgkmcnt(0)\n\ts_waitcnt vmcnt(11)" ::: "memory");
    LSTAMP(2 * s);
    __builtin_amdgcn_s_barrier();
    asm volatile("" ::: "memory");
    LSTAMP(2 * s + 1);
    __builtin_amdgcn_sched_barrier(0);
    rdst(F0, buf, 0, nbuf, 0, 6);
    mma(F1);
    K1_INTERLEAVE(6)
    __builtin_amdgcn_sched_barrier(0);
    rdst(F1, buf, 1, nbuf, 6, 5);
    mma(F0);
    K1_INTERLEAVE(5)
    __builtin_amdgcn_sched_barrier(0);
    nbuf = buf;
    buf = buf == 2 ? 0 : buf + 1;
  }
  STAMP(0, 2);
  asm volatile("s_waitcnt lgkmcnt(0)\n\ts_waitcnt vmcnt(11)" ::: "memory");
  __builtin_amdgcn_s_barrier();
  asm volatile("" ::: "memory");
  __builtin_amdgcn_sched_barrier(0);
  {
    const int gwv = min(blockIdx.x * 4 + w, 959);
#pragma unroll
    for (int i = 0; i < 2; ++i) {
      const char* bp = smem + K1_LDS + 6144 + w * 2048 + (4 * g + q4) * 64 + i * 32 + p4 * 8;
      s16x4 lo = __builtin_amdgcn_ds_read_tr16_b64_v4i16((lds_s16x4_ptr)(bp));
      s16x4 hi = __builtin_amdgcn_ds_read_tr16_b64_v4i16((lds_s16x4_ptr)(bp + 1024));
      bf16x8 v = __builtin_shufflevector(lo, hi, 0, 1, 2, 3, 4, 5, 6, 7);
      __builtin_amdgcn_raw_buffer_store_b128(__builtin_bit_cast(u32x4, v), rsO, lane * 16 + (int)OFF_W2F + (gwv * 2 + i) * 1024, 0, 16);
    }
  }
  __builtin_amdgcn_sched_barrier(0);
  rdst(F0, 2, 0, 0, 0, 0);
  mma(F1);
  K1_INTERLEAVE(0)
  __builtin_amdgcn_sched_barrier(0);
  rdst(F1, 2, 1, 0, 0, 0);
  mma(F0);
  K1_INTERLEAVE(0)
  __builtin_amdgcn_sched_barrier(0);
  asm volatile("s_waitcnt lgkmcnt(0)\n\ts_waitcnt vmcnt(2)" ::: "memory");
  __builtin_amdgcn_s_barrier();
  asm volatile("" ::: "memory");
  __builtin_amdgcn_sched_barrier(0);
  rdst(F0, 0, 0, 0, 0, 0);
  mma(F1);
  K1_INTERLEAVE(0)
  __builtin_amdgcn_sched_barrier(0);
  rdst(F1, 0, 1, 0, 0, 0);
  mma(F0);
  K1_INTERLEAVE(0)
  __builtin_amdgcn_sched_barrier(0);
  mma(F1);
  STAMP(0, 3);

  STAMP(0, 6);
  const int rt0 = rb * 8 + wm * 4;
#define K1AUX 16
  const int oHF = (int)OFF_HF + rt0 * 48 * 1024, oQF = (int)OFF_QF + rt0 * 20 * 1024;
  auto st16 = [&](int off, u32x4 v) { __builtin_amdgcn_raw_buffer_store_b128(v, rsO, lane * 16 + off, 0, K1AUX); };
  auto st8 = [&](int off, u32x2 v) { __builtin_amdgcn_raw_buffer_store_b64(v, rsO, lane * 16, off, 0); };
  u32x2 bias[7];
  {
    const char* bl = smem + K1_LDS + 1024 + w * 1024 + g * 8;
#pragma unroll
    for (int ni = 0; ni < 7; ++ni) {
      if (KV) { if (ni >= 5) bias[ni] = *(const u32x2*)(bl + 32 * (ni - 5)); }
      else if (ni >= 2) bias[ni] = *(const u32x2*)(bl + (ni < 6 ? 128 * wn + 32 * (ni - 2) : 256 + 32 * wn));
    }
  }
  auto addb = [&](const f32x4& a, const u32x2& b) -> f32x4 {
    return a + (f32x4){bf_lo(b[0]), bf_hi(b[0]), bf_lo(b[1]), bf_hi(b[1])};
  };

  if (!KV) {
#pragma unroll
    for (int mi = 0; mi < 4; ++mi) {
      u32x2 lo = cvt_q(acc[mi][0]), hi = cvt_q(acc[mi][1]);
      st16(oQF + (mi * 20 + 2 * j + wn) * 1024, (u32x4){lo[0], lo[1], hi[0], hi[1]});
    }
    STAMP(0, 7);
#pragma unroll
    for (int pr = 0; pr < 2; ++pr)
#pragma unroll
      for (int mi = 0; mi < 4; ++mi) {
        u32x2 lo = cvt_h(addb(acc[mi][2 + 2 * pr], bias[2 + 2 * pr])), hi = cvt_h(addb(acc[mi][3 + 2 * pr], bias[3 + 2 * pr]));
        st16(oHF + (mi * 48 + 5 * j + 2 * wn + pr) * 1024, (u32x4){lo[0], lo[1], hi[0], hi[1]});
      }
    STAMP(0, 8); STAMP(0, 9);
#pragma unroll
    for (int mi = 0; mi < 4; ++mi)
      st8(oHF + (mi * 48 + 5 * j + 4) * 1024 + wn * 8, cvt_h(addb(acc[mi][6], bias[6])));
  } else {
    asm volatile("s_waitcnt lgkmcnt(0)" ::: "memory");
    __builtin_amdgcn_s_barrier();
    asm volatile("" ::: "memory");
    STAMP(0, 7);
    char* xk = smem + (wm * 2) * 5 * 1024 + lane * 16;
    char* xv = xk + 20 * 1024;
    if (wn == 0) {
#pragma unroll
      for (int p = 0; p < 2; ++p) {
#pragma unroll
        for (int t = 0; t < 2; ++t) {
          u32x2 lo = cvt_v(acc[2 * p][3 + t]), hi = cvt_v(acc[2 * p + 1][3 + t]);
          *(u32x4*)(xv + (p * 5 + t) * 1024) = (u32x4){lo[0], lo[1], hi[0], hi[1]};
        }
#pragma unroll
        for (int t = 0; t < 3; ++t) {
          u32x2 lo = cvt_q(acc[2 * p][t]), hi = cvt_q(acc[2 * p + 1][t]);
          *(u32x4*)(xk + (p * 5 + t) * 1024) = (u32x4){lo[0], lo[1], hi[0], hi[1]};
        }
      }
    } else {
#pragma unroll
      for (int p = 0; p < 2; ++p) {
#pragma unroll
        for (int t = 0; t < 3; ++t) {
          u32x2 lo = cvt_v(acc[2 * p][2 + t]), hi = cvt_v(acc[2 * p + 1][2 + t]);
          *(u32x4*)(xv + (p * 5 + 2 + t) * 1024) = (u32x4){lo[0], lo[1], hi[0], hi[1]};
        }
#pragma unroll
        for (int t = 0; t < 2; ++t) {
          u32x2 lo = cvt_q(acc[2 * p][t]), hi = cvt_q(acc[2 * p + 1][t]);
          *(u32x4*)(xk + (p * 5 + 3 + t) * 1024) = (u32x4){lo[0], lo[1], hi[0], hi[1]};
        }
      }
    }
    asm volatile("s_waitcnt lgkmcnt(0)" ::: "memory");
    __builtin_amdgcn_s_barrier();
    asm volatile("" ::: "memory");
    STAMP(0, 8);
    const int a0 = w == 0 ? 0 : w - 1;
    bf16x8 pkA[4], vA3[3][4], pkB[4], vB[4];
#pragma unroll
    for (int ks4 = 0; ks4 < 4; ++ks4) {
      pkA[ks4] = *(const bf16x8*)(smem + (ks4 * 5 + w) * 1024 + lane * 16);
#pragma unroll
      for (int t = 0; t < 3; ++t) vA3[t][ks4] = *(const bf16x8*)(smem + (20 + ks4 * 5 + a0 + t) * 1024 + lane * 16);
    }
    const bool hasB = (w == 0 || w == 3);
    const int teB = w == 0 ? 4 : 3;
    if (hasB) {
#pragma unroll
      for (int ks4 = 0; ks4 < 4; ++ks4) {
        pkB[ks4] = *(const bf16x8*)(smem + (ks4 * 5 + 4) * 1024 + lane * 16);
        vB[ks4] = *(const bf16x8*)(smem + (20 + ks4 * 5 + teB) * 1024 + lane * 16);
      }
    }
    if (wn == 0) {
#pragma unroll
      for (int mi = 0; mi < 4; ++mi) {
        u32x2 lo = cvt_h(addb(acc[mi][5], bias[5])), hi = cvt_h(addb(acc[mi][6], bias[6]));
        st16(oHF + (mi * 48 + 40 + j) * 1024, (u32x4){lo[0], lo[1], hi[0], hi[1]});
      }
    } else {
#pragma unroll
      for (int mi = 0; mi < 4; ++mi)
        st8(oQF + (mi * 20 + 16 + (j >> 1)) * 1024 + (j & 1) * 8, cvt_q(acc[mi][5]));
    }
    STAMP(0, 9);
    u16* KVP = (u16*)(ws + OFF_KVP) + (size_t)j * 4 * 6400 + rb * 400;
    u16* ZP = (u16*)(ws + OFF_ZP) + j * 80 * 16 + rb;
    const bf16x8 ones = {0x3f80, 0x3f80, 0x3f80, 0x3f80, 0x3f80, 0x3f80, 0x3f80, 0x3f80};
    auto put_kv = [&](const f32x4& d, int te, int td) {
      const int dg = 16 * td + c, eg = 16 * te + 4 * g;
      const int hd = dg / 20, he = eg / 20;
      if (hd == he) *(u32x2*)(KVP + hd * 6400 + (dg - hd * 20) * 20 + (eg - he * 20)) = cvt_v(d);
    };
    auto put_z = [&](const f32x4& d, int td) {
      if (g == 0) ZP[(16 * td + c) * 16] = (u16)(pack2(d[0], 0.f) & 0xffffu);
    };
    f32x4 dA[4];
#pragma unroll
    for (int t = 0; t < 4; ++t) dA[t] = (f32x4){0.f, 0.f, 0.f, 0.f};
#pragma unroll
    for (int ks4 = 0; ks4 < 4; ++ks4) {
#pragma unroll
      for (int t = 0; t < 3; ++t) dA[t] = MFMA16(vA3[t][ks4], pkA[ks4], dA[t]);
      dA[3] = MFMA16(ones, pkA[ks4], dA[3]);
    }
#pragma unroll
    for (int t = 0; t < 3; ++t) put_kv(dA[t], a0 + t, w);
    put_z(dA[3], w);
    if (hasB) {
      f32x4 dB = {0.f, 0.f, 0.f, 0.f}, dZ = {0.f, 0.f, 0.f, 0.f};
#pragma unroll
      for (int ks4 = 0; ks4 < 4; ++ks4) { dB = MFMA16(vB[ks4], pkB[ks4], dB); dZ = MFMA16(ones, pkB[ks4], dZ); }
      put_kv(dB, teB, 4);
      if (w == 0) put_z(dZ, 4);
    }
  }
}

__global__ __launch_bounds__(256, 2) void k1_kernel(const u16* __restrict__ x, const u16* __restrict__ wqkv,
                                                   const u16* __restrict__ w1, const u16* __restrict__ b1,
                                                   const u16* __restrict__ w2, const u16* __restrict__ gate_w,
                                                   char* __restrict__ ws, const char* outp, const char* wop,
                                                   const char* b2p) {
  extern __shared__ __attribute__((aligned(16))) char smem[];
  const int bid = blockIdx.x;
  const int xcd = bid & 7, idx = bid >> 3;
  const int ri = idx & 7, ci = idx >> 3;
  const int rb = (xcd & 1) * 8 + ri;
  const int cq = (xcd >> 1) * 4 + ci;
  if (bid == 0 && threadIdx.x < 64) ((unsigned*)(ws + OFF_FLAG))[threadIdx.x * 64] = 0u;
  if (idx < 5 && threadIdx.x < 64) {
    const char* pc = (const char*)(__builtin_amdgcn_s_getpc() & ~255ull);
    __builtin_amdgcn_global_load_lds((const unsigned*)(pc + min(idx * 64 + (int)threadIdx.x, 289) * 128), (unsigned*)(smem + K1_LDS), 4, 0, 0);
  }
  if (idx >= 5 && idx < 10 && threadIdx.x < 64) {
    const int t = (idx - 5) * 64 + (int)threadIdx.x;
    constexpr int NWS = (int)((OFF_FLAG + 16384 + 65535) >> 16);
    const char* p = t < NWS ? ws + (size_t)t * 65536
                  : (t < NWS + 40 ? outp + (size_t)(t - NWS) * 65536
                  : (t < NWS + 53 ? wop + (size_t)(t - NWS - 40) * 65536 : b2p));
    __builtin_amdgcn_global_load_lds((const unsigned*)p, (unsigned*)(smem + K1_LDS), 4, 0, 0);
  }
  if (cq & 1) k1_body<false>(smem, rb, cq >> 1, ci, ri, x, wqkv, w1, b1, w2, gate_w, ws);
  else k1_body<true>(smem, rb, cq >> 1, ci, ri, x, wqkv, w1, b1, w2, gate_w, ws);
  STAMP(0, 4);
  STAMP(0, 5); RSTAMP(0, 31);
}

typedef __attribute__((address_space(1))) unsigned gu32;
#define RLX_AGENT __ATOMIC_RELAXED, __HIP_MEMORY_SCOPE_AGENT
typedef __attribute__((address_space(3))) unsigned lu32;
DI void publish(char* ws, lu32* arrive, int u, int lane) {
  asm volatile("s_waitcnt vmcnt(0)" ::: "memory");
  if (lane == 0) {
    if (__hip_atomic_fetch_add(arrive, 1u, __ATOMIC_RELAXED, __HIP_MEMORY_SCOPE_WORKGROUP) == 1u) {
      __hip_atomic_fetch_add((gu32*)(ws + OFF_FLAG) + (u >> 3) * 64, 1u, RLX_AGENT);
      __hip_atomic_fetch_add((gu32*)(ws + OFF_FLAG) + (32 + (u & 7)) * 64, 1u, RLX_AGENT);
    }
  }
}
DI void task_w(int u, int lane, char* lds  , lu32* arrive, const u16* __restrict__ wo, char* ws) {
  const __amdgpu_buffer_rsrc_t rs = __builtin_amdgcn_make_buffer_rsrc(ws, 0, (int)OFF_FLAG, 0x00020000);
  const int h = u >> 3, cr = u & 7;
  const int g = lane >> 4, c = lane & 15;
  const u16* KVP = (const u16*)(ws + OFF_KVP) + h * 6400 + min(lane, 49) * 8;
  u32x4 kp[16];
#pragma unroll
  for (int p = 0; p < 16; ++p) kp[p] = *(const u32x4*)(KVP + p * 400);
#pragma unroll
  for (int i = 0; i < 4; ++i) {
    const int q = min(i * 64 + lane, 199), row = q / 10, ch = q - row * 10;
    __builtin_amdgcn_global_load_lds((const unsigned*)(wo + (size_t)(h * 20 + row) * DM + cr * 80 + ch * 8),
                                     (unsigned*)(lds + 2048 + i * 1024), 16, 0, 0);
  }
  __builtin_amdgcn_sched_barrier(0);
  __builtin_amdgcn_s_barrier();
  __builtin_amdgcn_sched_barrier(0);
  STAMP(2, 8);
  f32x4 s0 = {0.f, 0.f, 0.f, 0.f}, s1 = {0.f, 0.f, 0.f, 0.f};
#pragma unroll
  for (int p = 0; p < 16; ++p) {
    s0 += (f32x4){bf_lo(kp[p][0]), bf_hi(kp[p][0]), bf_lo(kp[p][1]), bf_hi(kp[p][1])};
    s1 += (f32x4){bf_lo(kp[p][2]), bf_hi(kp[p][2]), bf_lo(kp[p][3]), bf_hi(kp[p][3])};
  }
  asm volatile("s_waitcnt vmcnt(0)" ::: "memory");
  STAMP(2, 9);
  bf16x8 wf[5];
  {
    const int r0 = min(8 * g + (c >> 2), 19), r1 = min(8 * g + 4 + (c >> 2), 19);
#pragma unroll
    for (int t = 0; t < 5; ++t) {
      s16x4 lo = __builtin_amdgcn_ds_read_tr16_b64_v4i16((lds_s16x4_ptr)(lds + 2048 + r0 * 160 + t * 32 + (c & 3) * 8));
      s16x4 hi = __builtin_amdgcn_ds_read_tr16_b64_v4i16((lds_s16x4_ptr)(lds + 2048 + r1 * 160 + t * 32 + (c & 3) * 8));
      wf[t] = __builtin_shufflevector(lo, hi, 0, 1, 2, 3, 4, 5, 6, 7);
    }
  }
  *(u32x4*)(lds + lane * 32) = (u32x4){0u, 0u, 0u, 0u};
  *(u32x4*)(lds + lane * 32 + 16) = (u32x4){0u, 0u, 0u, 0u};
  __builtin_amdgcn_s_waitcnt(0xC07F);
  if (lane < 50) {
    const int i0 = lane * 8, d0 = i0 / 20, e0 = i0 - d0 * 20;
    *(u32x2*)(lds + d0 * 64 + e0 * 2) = (u32x2){pack2(s0[0], s0[1]), pack2(s0[2], s0[3])};
    const int i1 = i0 + 4, d1 = i1 / 20, e1 = i1 - d1 * 20;
    *(u32x2*)(lds + d1 * 64 + e1 * 2) = (u32x2){pack2(s1[0], s1[1]), pack2(s1[2], s1[3])};
  }
  __builtin_amdgcn_s_waitcnt(0xC07F);
  bf16x8 ka0 = *(const bf16x8*)(lds + c * 64 + g * 16);
  bf16x8 ka1 = *(const bf16x8*)(lds + (16 + c) * 64 + g * 16);
#pragma unroll
  for (int t = 0; t < 5; ++t) {
    f32x4 d0 = MFMA16(ka0, wf[t], ((f32x4){0.f, 0.f, 0.f, 0.f}));
    f32x4 d1 = MFMA16(ka1, wf[t], ((f32x4){0.f, 0.f, 0.f, 0.f}));
    {
      const int k = h * 20 + 4 * g;
      const int kb = k >> 5, half = (k >> 4) & 1, gg = (k & 15) >> 2;
      __builtin_amdgcn_raw_buffer_store_b64((u32x2){pack2(d0[0], d0[1]), pack2(d0[2], d0[3])}, rs,
          (int)OFF_WPF + (kb * 40 + cr * 5 + t) * 1024 + half * 512 + (gg * 16 + c) * 8, 0, 16);
    }
    if (g == 0) {
      const int k = h * 20 + 16;
      const int kb = k >> 5, half = (k >> 4) & 1, gg = (k & 15) >> 2;
      __builtin_amdgcn_raw_buffer_store_b64((u32x2){pack2(d1[0], d1[1]), pack2(d1[2], d1[3])}, rs,
          (int)OFF_WPF + (kb * 40 + cr * 5 + t) * 1024 + half * 512 + (gg * 16 + c) * 8, 0, 16);
    }
  }
  STAMP(2, 10);
  publish(ws, arrive, u, lane);
}

DI void task_a(int u, int lane, char* lds  , lu32* arrive, char* ws) {
  const __amdgpu_buffer_rsrc_t rs = __builtin_amdgcn_make_buffer_rsrc(ws, 0, (int)OFF_FLAG, 0x00020000);
  const int rt = u >> 1, kh = u & 1;
  const int g = lane >> 4, c = lane & 15;
  const u16* ZP = (const u16*)(ws + OFF_ZP) + kh * 320 * 16;
  u32x4 zp[10];
#pragma unroll
  for (int i = 0; i < 10; ++i) zp[i] = *(const u32x4*)(ZP + i * 512 + lane * 8);
  const char* QF = ws + OFF_QF + ((size_t)rt * 20 + kh * 10) * 1024;
  u32x4 qf[10];
#pragma unroll
  for (int b = 0; b < 10; ++b) qf[b] = *(const u32x4*)(QF + b * 1024 + lane * 16);
  const float gate = ((const float*)(ws + OFF_G))[rt * 16 + c];
  __builtin_amdgcn_sched_barrier(0);
  __builtin_amdgcn_s_barrier();
  __builtin_amdgcn_sched_barrier(0);
  STAMP(2, 8);
#pragma unroll
  for (int i = 0; i < 10; ++i) {
    float t = ((bf_lo(zp[i][0]) + bf_hi(zp[i][0])) + (bf_lo(zp[i][1]) + bf_hi(zp[i][1]))) +
              ((bf_lo(zp[i][2]) + bf_hi(zp[i][2])) + (bf_lo(zp[i][3]) + bf_hi(zp[i][3])));
    t += __builtin_bit_cast(float, __builtin_amdgcn_update_dpp(0, __builtin_bit_cast(int, t), 0xB1, 0xF, 0xF, true));
    if ((lane & 1) == 0) *(u16*)(lds + (i * 32 + (lane >> 1)) * 2) = (u16)(pack2(t, 0.f) & 0xffffu);
  }
  STAMP(2, 9);
  __builtin_amdgcn_s_waitcnt(0xC07F);
  f32x4 den = {0.f, 0.f, 0.f, 0.f};
#pragma unroll
  for (int b = 0; b < 10; ++b) {
    const int k0 = 32 * b + 4 * g, k1 = k0 + 16;
    u32x2 zl = *(const u32x2*)(lds + k0 * 2), zh = *(const u32x2*)(lds + k1 * 2);
    const bool m0 = (k0 / 20) == c, m1 = (k1 / 20) == c;
    u32x4 zf = {m0 ? zl[0] : 0u, m0 ? zl[1] : 0u, m1 ? zh[0] : 0u, m1 ? zh[1] : 0u};
    den = MFMA16(__builtin_bit_cast(bf16x8, zf), __builtin_bit_cast(bf16x8, qf[b]), den);
  }
  f32x4 rd;
#pragma unroll
  for (int r = 0; r < 4; ++r) rd[r] = gate / (den[r] + 1e-6f);
  *(f32x4*)(lds + 1024 + c * 64 + g * 16) = rd;
  __builtin_amdgcn_s_waitcnt(0xC07F);
#pragma unroll
  for (int b = 0; b < 10; ++b) {
    const int k0 = 32 * b + 4 * g, k1 = k0 + 16;
    const float r0 = *(const float*)(lds + 1024 + c * 64 + (k0 / 20) * 4);
    const float r1 = *(const float*)(lds + 1024 + c * 64 + (k1 / 20) * 4);
    u32x4 o;
    o[0] = pack2(bf_lo(qf[b][0]) * r0, bf_hi(qf[b][0]) * r0);
    o[1] = pack2(bf_lo(qf[b][1]) * r0, bf_hi(qf[b][1]) * r0);
    o[2] = pack2(bf_lo(qf[b][2]) * r1, bf_hi(qf[b][2]) * r1);
    o[3] = pack2(bf_lo(qf[b][3]) * r1, bf_hi(qf[b][3]) * r1);
    __builtin_amdgcn_raw_buffer_store_b128(o, rs, (int)OFF_AF + (rt * 20 + kh * 10 + b) * 1024 + lane * 16, 0, 16);
  }
  STAMP(2, 10);
  publish(ws, arrive, u, lane);
}

#define K2D 2
constexpr int K2_DEPTH = K2D;
__global__ __launch_bounds__(384, 2) void k2_kernel(const u16* __restrict__ wo, const u16* __restrict__ b2,
                                                   char* ws, u16* __restrict__ out) {
  __shared__ __attribute__((aligned(16))) char red[4 * 20 * 1024 + 8192 + 16];
  lu32* ready = (lu32*)(red + 81920 + 8192);
  const int bid = blockIdx.x;
  const int xcd = bid & 7, idx = bid >> 3;
  const int rb = (xcd >> 1) * 8 + (idx & 7);
  const int cb = (xcd & 1) * 4 + (idx >> 3);
  const int lane = threadIdx.x & 63;
  const int w = __builtin_amdgcn_readfirstlane(threadIdx.x >> 6);
  const int g = lane >> 4, c = lane & 15;

  if (w >= 4) {
    __builtin_amdgcn_s_setprio(3);
    RSTAMP(2, 30); STAMP(2, 0);
    if (w == 4) {
      if (lane < 3) __hip_atomic_store(ready + lane, 0u, __ATOMIC_RELAXED, __HIP_MEMORY_SCOPE_WORKGROUP);
      asm volatile("s_waitcnt lgkmcnt(0)" ::: "memory");
      task_w(bid, lane, red + 81920, ready + 2, wo, ws);
      STAMP(2, 1);
    } else {
      task_a(bid, lane, red + 81920 + 6144, ready + 2, ws);
      STAMP(2, 1);
      gu32* cntA = (gu32*)(ws + OFF_FLAG) + rb * 64;
      gu32* cntW = (gu32*)(ws + OFF_FLAG) + (32 + cb) * 64;
      for (unsigned spins = 0; spins < (1u << 24); ++spins) {
        if (__builtin_amdgcn_readfirstlane(__hip_atomic_load(ready + 1, __ATOMIC_RELAXED, __HIP_MEMORY_SCOPE_WORKGROUP)) != 0u) break;
        __builtin_amdgcn_s_sleep(2);
      }
      for (unsigned spins = 0; spins < (1u << 22); ++spins) {
        const unsigned a = __hip_atomic_load(cntA, RLX_AGENT), b = __hip_atomic_load(cntW, RLX_AGENT);
        if (__builtin_amdgcn_readfirstlane(a) == 8u && __builtin_amdgcn_readfirstlane(b) == 32u) break;
        __builtin_amdgcn_s_sleep(4);
      }
      if (lane == 0) __hip_atomic_store(ready, 1u, __ATOMIC_RELAXED, __HIP_MEMORY_SCOPE_WORKGROUP);
    }
    STAMP(2, 2); STAMP(2, 3); STAMP(2, 4); STAMP(2, 5); RSTAMP(2, 31);
    return;
  }

  const __amdgpu_buffer_rsrc_t rs = __builtin_amdgcn_make_buffer_rsrc(ws, 0, (int)OFF_FLAG, 0x00020000);
  const int voff = lane * 16;
  const int oH = (int)OFF_HF + (rb * 4 * 48 + w) * 1024;
  const int oA = (int)OFF_AF + (rb * 4 * 20 + w) * 1024;
  const int oW2 = (int)OFF_W2F + (w * 40 + cb * 5) * 1024;
  const int oWP = (int)OFF_WPF + (w * 40 + cb * 5) * 1024;

  RSTAMP(2, 30); STAMP(2, 0);
  f32x4 acc[4][5];
#pragma unroll
  for (int mi = 0; mi < 4; ++mi)
#pragma unroll
    for (int ni = 0; ni < 5; ++ni) acc[mi][ni] = (f32x4){0.f, 0.f, 0.f, 0.f};

  constexpr int NS = K2_DEPTH + 1;
  u32x4 fa[NS][4], fb[NS][5];
  float gv[4];
  u32x2 b2v[5];
#define K2ROTR 0
#define K2ROTC 0
  const int rot = (((idx & 7) * K2ROTR) / 2 + ((idx >> 3) * K2ROTC) / 2) % 12;
  auto load = [&](int i, int slot) {
    if (i < 12) {
      int ir = i + rot; ir = ir >= 12 ? ir - 12 : ir;
#pragma unroll
      for (int ni = 0; ni < 5; ++ni) fb[slot][ni] = __builtin_amdgcn_raw_buffer_load_b128(rs, voff, oW2 + (4 * ir * 40 + ni) * 1024, 0);
#pragma unroll
      for (int mi = 0; mi < 4; ++mi) fa[slot][mi] = __builtin_amdgcn_raw_buffer_load_b128(rs, voff, oH + (mi * 48 + 4 * ir) * 1024, 0);
    } else {
      const int ii = i - 12;
#pragma unroll
      for (int ni = 0; ni < 5; ++ni) {
        u32x2 lo = __builtin_amdgcn_raw_buffer_load_b64(rs, lane * 8, oWP + (4 * ii * 40 + ni) * 1024, 16);
        u32x2 hi = __builtin_amdgcn_raw_buffer_load_b64(rs, lane * 8, oWP + (4 * ii * 40 + ni) * 1024 + 512, 16);
        fb[slot][ni] = (u32x4){lo[0], lo[1], hi[0], hi[1]};
      }
#pragma unroll
      for (int mi = 0; mi < 4; ++mi) fa[slot][mi] = __builtin_amdgcn_raw_buffer_load_b128(rs, voff, oA + (mi * 20 + 4 * ii) * 1024, 16);
    }
  };
  __builtin_amdgcn_s_barrier();
  __builtin_amdgcn_sched_barrier(0);
#pragma unroll
  for (int d = 0; d < K2_DEPTH; ++d) load(d, d);
#pragma unroll
  for (int mi = 0; mi < 4; ++mi)
    gv[mi] = __uint_as_float(__builtin_amdgcn_raw_buffer_load_b32(rs, c * 4, (int)OFF_G + (rb * 64 + mi * 16) * 4, 0));
  __builtin_amdgcn_sched_barrier(0);
#pragma unroll
  for (int i = 0; i < 17; ++i) {
    const int slot = i % NS;
    if (i == 1) { STAMP(2, 1); }
#define K2GO 6
    if (i == K2GO) {
      if (lane == 0) __hip_atomic_store(ready + 1, 1u, __ATOMIC_RELAXED, __HIP_MEMORY_SCOPE_WORKGROUP);
      __builtin_amdgcn_sched_barrier(0);
    }
    if (i == 12 - K2_DEPTH) {
      STAMP(2, 6);
      for (unsigned spins = 0; spins < (1u << 24); ++spins) {
        if (__builtin_amdgcn_readfirstlane(__hip_atomic_load(ready, __ATOMIC_RELAXED, __HIP_MEMORY_SCOPE_WORKGROUP)) != 0u) break;
        __builtin_amdgcn_s_sleep(1);
      }
      asm volatile("" ::: "memory");
      STAMP(2, 7);
      __builtin_amdgcn_sched_barrier(0);
    }
    if (i == 12) {
      STAMP(2, 2);
#pragma unroll
      for (int mi = 0; mi < 4; ++mi) {
        gv[mi] = 1.0f - gv[mi];
#pragma unroll
        for (int ni = 0; ni < 5; ++ni) acc[mi][ni] *= gv[mi];
      }
      __builtin_amdgcn_sched_barrier(0);
    }
    if (i + K2_DEPTH < 17) load(i + K2_DEPTH, (i + K2_DEPTH) % NS);
    if (i == 15) {
#pragma unroll
      for (int ni = 0; ni < 5; ++ni) b2v[ni] = *(const u32x2*)(b2 + cb * 80 + ni * 16 + 4 * g);
      __builtin_amdgcn_sched_barrier(0);
    }
#pragma unroll
    for (int mi = 0; mi < 4; ++mi)
#pragma unroll
      for (int ni = 0; ni < 5; ++ni)
        acc[mi][ni] = MFMA16(__builtin_bit_cast(bf16x8, fb[slot][ni]), __builtin_bit_cast(bf16x8, fa[slot][mi]), acc[mi][ni]);
    if (i + K2_DEPTH < 12) {
#pragma unroll
      for (int k = 0; k < 9; ++k) {
        __builtin_amdgcn_sched_group_barrier(0x008, 2, 0);
        __builtin_amdgcn_sched_group_barrier(0x020, 1, 0);
      }
      __builtin_amdgcn_sched_group_barrier(0x008, 2, 0);
    } else if (i + K2_DEPTH < 17) {
#pragma unroll
      for (int k = 0; k < 14; ++k) {
        __builtin_amdgcn_sched_group_barrier(0x008, 1, 0);
        __builtin_amdgcn_sched_group_barrier(0x020, 1, 0);
      }
      __builtin_amdgcn_sched_group_barrier(0x008, 6, 0);
    }
    __builtin_amdgcn_sched_barrier(0);
  }

  STAMP(2, 3);
#pragma unroll
  for (int mi = 0; mi < 4; ++mi)
    if (mi != w) {
#pragma unroll
      for (int ni = 0; ni < 5; ++ni) *(f32x4*)(red + ((w * 4 + mi) * 5 + ni) * 1024 + lane * 16) = acc[mi][ni];
    }
  __syncthreads();
  float gw = gv[0];
  f32x4 mine[5];
#pragma unroll
  for (int ni = 0; ni < 5; ++ni) mine[ni] = acc[0][ni];
#pragma unroll
  for (int mi = 1; mi < 4; ++mi)
    if (w == mi) {
      gw = gv[mi];
#pragma unroll
      for (int ni = 0; ni < 5; ++ni) mine[ni] = acc[mi][ni];
    }
#pragma unroll
  for (int ni = 0; ni < 5; ++ni) {
    f32x4 s = mine[ni];
#pragma unroll
    for (int o = 1; o < 4; ++o) {
      const int ow = (w + o) & 3;
      s += *(const f32x4*)(red + ((ow * 4 + w) * 5 + ni) * 1024 + lane * 16);
    }
    const int n = cb * 80 + ni * 16 + 4 * g;
    const u32x2 bv = b2v[ni];
    s[0] += gw * bf_lo(bv[0]); s[1] += gw * bf_hi(bv[0]); s[2] += gw * bf_lo(bv[1]); s[3] += gw * bf_hi(bv[1]);
    *(u32x2*)(out + (size_t)(rb * 64 + w * 16 + c) * DM + n) = (u32x2){pack2(s[0], s[1]), pack2(s[2], s[3])};
  }
  STAMP(2, 4);
  STAMP(2, 5); RSTAMP(2, 31);
}

extern "C" void kernel_launch(void* const* d_in, const int* in_sizes, int n_in, void* d_out, int out_size,
                              void* d_ws, size_t ws_size, hipStream_t stream) {
  const u16* x = (const u16*)d_in[0];
  const u16* wqkv = (const u16*)d_in[1];
  const u16* wo = (const u16*)d_in[2];
  const u16* w1 = (const u16*)d_in[4];
  const u16* b1 = (const u16*)d_in[5];
  const u16* w2 = (const u16*)d_in[6];
  const u16* b2 = (const u16*)d_in[7];
  const u16* gw = (const u16*)d_in[8];
  char* ws = (char*)d_ws;
  (void)hipFuncSetAttribute((const void*)k1_kernel, hipFuncAttributeMaxDynamicSharedMemorySize, K1_LDS + 14336);
  k1_kernel<<<256, 256, K1_LDS + 14336, stream>>>(x, wqkv, w1, b1, w2, gw, ws, (const char*)d_out, (const char*)wo, (const char*)b2);
  k2_kernel<<<256, 384, 0, stream>>>(wo, b2, ws, (u16*)d_out);
}
```

```cpp
#include <hip/hip_runtime.h>
#include <hip/hip_bf16.h>

typedef unsigned short u16;
typedef short bf16x8 __attribute__((ext_vector_type(8)));
typedef short s16x4 __attribute__((ext_vector_type(4)));
typedef float f32x4 __attribute__((ext_vector_type(4)));
typedef unsigned u32x4 __attribute__((ext_vector_type(4)));
typedef unsigned u32x2 __attribute__((ext_vector_type(2)));
typedef __attribute__((address_space(3))) s16x4* lds_s16x4_ptr;
#define DI __device__ __forceinline__
#define MFMA16(a, b, c) __builtin_amdgcn_mfma_f32_16x16x32_bf16((a), (b), (c), 0, 0, 0)

constexpr int SEQ = 2048, DM = 640, FF = 1536, NQKV = 1920;

constexpr size_t OFF_HF = 0;
constexpr size_t OFF_QF = OFF_HF + 128ull * 48 * 1024;
constexpr size_t OFF_AF = OFF_QF + 128ull * 20 * 1024;
constexpr size_t OFF_W2F = OFF_AF + 128ull * 20 * 1024;
constexpr size_t OFF_WPF = OFF_W2F + 48ull * 40 * 1024;
constexpr size_t OFF_KVP = OFF_WPF + 20ull * 40 * 1024;
constexpr size_t OFF_ZP = OFF_KVP + 16ull * 32 * 1600;
constexpr size_t OFF_G = OFF_ZP + 16ull * 640 * 4;
constexpr size_t OFF_FLAG = OFF_G + 2048 * 4;
constexpr size_t OFF_DBG = 64ull << 20;
#define STAMP(kern, idx)
#define RSTAMP(kern, idx)
#define LSTAMP(idx)

DI unsigned pack2(float lo, float hi) {
  typedef __bf16 bf2 __attribute__((ext_vector_type(2)));
  typedef float f2 __attribute__((ext_vector_type(2)));
  f2 v = {lo, hi};
  bf2 r = __builtin_convertvector(v, bf2);
  return __builtin_bit_cast(unsigned, r);
}
DI float bf_lo(unsigned u) { return __uint_as_float(u << 16); }
DI float bf_hi(unsigned u) { return __uint_as_float(u & 0xffff0000u); }
DI float rbf(float f) { return bf_lo(pack2(f, 0.f) & 0xffffu); }
DI float phi(float t) {
  float e = __builtin_amdgcn_exp2f(t * 1.4426950408889634f);
  e = fminf(e, 1.0f);
  return fmaxf(t, 0.f) + e + 1e-6f;
}

constexpr int K1_STAGE = 45056;
constexpr int K1_LDS = 3 * K1_STAGE;

struct ColSrc { const u16* p; int stride; };
template <bool KV> DI ColSrc pos_src(int pos, int j, const u16* wqkv, const u16* w1) {
  ColSrc s;
  if (KV) {
    if (pos < 5) { s.p = wqkv + 640 + 80 * j + 16 * pos; s.stride = NQKV; }
    else if (pos == 5) { s.p = wqkv + 16 * (32 + j); s.stride = NQKV; }
    else if (pos < 11) { s.p = wqkv + 1280 + 80 * j + 16 * (pos - 6); s.stride = NQKV; }
    else if (pos == 11) { s.p = wqkv + 1280 + 80 * j + 64; s.stride = NQKV; }
    else { s.p = w1 + 32 * (40 + j) + 16 * (pos - 12); s.stride = FF; }
  } else {
    if (pos < 2) { s.p = wqkv + 32 * (2 * j) + 16 * pos; s.stride = NQKV; }
    else if (pos < 6) { s.p = w1 + 32 * (5 * j) + 16 * (pos - 2); s.stride = FF; }
    else if (pos < 8) { s.p = wqkv + 32 * (2 * j + 1) + 16 * (pos - 6); s.stride = NQKV; }
    else { s.p = w1 + 32 * (5 * j + 2) + 16 * (pos - 8); s.stride = FF; }
  }
  return s;
}
template <bool KV> DI int slot_pos(int wn, int ni) {
  if (KV) {
    const int p0 = ni < 3 ? ni : (ni < 5 ? ni + 3 : ni + 7);
    const int p1 = ni < 2 ? ni + 3 : (ni < 5 ? ni + 6 : (ni == 5 ? 5 : 11));
    return wn ? p1 : p0;
  }
  return ni < 6 ? 6 * wn + ni : 12 + wn;
}
DI float relu_f(float v) { return __int_as_float(max(__float_as_int(v), 0)); }
DI float phi1(float t1) {
  float e = __builtin_amdgcn_exp2f(__builtin_fmaf(t1, 1.4426950408889634f, -1.4426950408889634e-6f));
  e = __builtin_amdgcn_fmed3f(e, 0.0f, 1.0f);
  return __int_as_float(max(__float_as_int(t1), __float_as_int(1e-6f))) + e;
}
DI u32x2 cvt_q(const f32x4& a) { return (u32x2){pack2(phi1(a[0]), phi1(a[1])), pack2(phi1(a[2]), phi1(a[3]))}; }
DI u32x2 cvt_h(const f32x4& a) {
  float t0 = relu_f(a[0]), t1 = relu_f(a[1]), t2 = relu_f(a[2]), t3 = relu_f(a[3]);
  return (u32x2){pack2(t0 * t0, t1 * t1), pack2(t2 * t2, t3 * t3)};
}
DI u32x2 cvt_v(const f32x4& a) { return (u32x2){pack2(a[0], a[1]), pack2(a[2], a[3])}; }

template <bool KV>
DI void k1_body(char* smem, int rb, int j, int ci, int ri, const u16* __restrict__ x, const u16* __restrict__ wqkv,
                const u16* __restrict__ w1, const u16* __restrict__ b1, const u16* __restrict__ w2,
                const u16* __restrict__ gate_w, char* __restrict__ ws, const char* wop) {
  const int tid = threadIdx.x;
  const int lane = tid & 63;
  const int w = __builtin_amdgcn_readfirstlane(tid >> 6);
  const int wm = w >> 1, wn = w & 1;
  const int g = lane >> 4, c = lane & 15;
  RSTAMP(0, 30); STAMP(0, 0);

  const __amdgpu_buffer_rsrc_t rsX = __builtin_amdgcn_make_buffer_rsrc((void*)x, 0, SEQ * DM * 2, 0x00020000);
  const __amdgpu_buffer_rsrc_t rsQ = __builtin_amdgcn_make_buffer_rsrc((void*)wqkv, 0, DM * NQKV * 2, 0x00020000);
  const __amdgpu_buffer_rsrc_t rsW = __builtin_amdgcn_make_buffer_rsrc((void*)w1, 0, DM * FF * 2, 0x00020000);
  const int vA = (lane >> 3) * (DM * 2) + (((lane & 7) ^ ((lane >> 3) & 7)) * 16);
#define NCOM 2
  const int rot = NCOM ? (ri + 2 * ci) % (10 - NCOM) : (ri + (5 * ci) / 2) % 10;
  int ks = NCOM ? 0 : rot, cnt = 0;
  int sA = (rb * 128 + 32 * w) * (DM * 2) + ks * 128;
  const int r16 = lane >> 2;
  const int cs = (lane & 3) ^ (((r16 >> 3) & 1) << 1);
  const int vQ = r16 * (NQKV * 2) + cs * 16;
  const int vW = r16 * (FF * 2) + cs * 16;
  const int vS = r16 * (NQKV * 2) + (cs & 1) * 16 + ((cs >> 1) ? 0 : ((640 + 80 * j + 64) - 16 * (32 + j)) * 2);
  const int vU = r16 * (NQKV * 2) + (cs & 1) * 16;
  int sQ = (w * 16 + ks * 64) * (NQKV * 2), sW = (w * 16 + ks * 64) * (FF * 2);
  auto st1 = [&](int buf, int i) {
    char* base = smem + buf * K1_STAGE;
    typedef __attribute__((address_space(3))) void* ldsp;
    if (i < 4) {
      __builtin_amdgcn_raw_ptr_buffer_load_lds(rsX, (ldsp)(base + (w * 4 + i) * 1024), 16, vA, sA + i * 8 * DM * 2, 0, 0);

    } else {
      const int p = i - 4;
      ldsp dst = (ldsp)(base + 16384 + (w * 7 + p) * 1024);
      const int col = (int)(pos_src<KV>((KV && p == 2) ? 5 : 2 * p, j, (const u16*)0, (const u16*)0).p - (const u16*)0) * 2;
      const bool isW = KV ? (p == 6) : !(p == 0 || p == 3);
      if (isW) __builtin_amdgcn_raw_ptr_buffer_load_lds(rsW, dst, 16, vW, sW + col, 0, 0);
      else __builtin_amdgcn_raw_ptr_buffer_load_lds(rsQ, dst, 16, (KV && p == 2) ? vS : ((KV && p == 5) ? vU : vQ), sQ + col, 0, 0);
      if (p == 6) {
        ++cnt;
        const int adv = cnt == NCOM ? 1 + rot : (ks == 9 ? -(9 - NCOM) : 1);
        ks += adv; sA += adv * 128; sQ += adv * (64 * NQKV * 2); sW += adv * (64 * FF * 2);
      }
    }
  };
  asm volatile("" ::: "memory");
  const int grow = rb * 128 + j * 16 + w * 4 + g;
  u32x4 gx[5], gwv[5];
  if (!KV) {
    const u16* xp = x + (size_t)grow * DM + c * 8;
    const u16* gp = gate_w + c * 8;
#pragma unroll
    for (int i = 0; i < 5; ++i) {
      asm volatile("global_load_dwordx4 %0, %1, off offset:%2" : "=&v"(gx[i]) : "v"(xp), "n"(i * 256) : "memory");
      asm volatile("global_load_dwordx4 %0, %1, off offset:%2" : "=&v"(gwv[i]) : "v"(gp), "n"(i * 256) : "memory");
    }
  }
#pragma unroll
  for (int i = 0; i < 11; ++i) st1(0, i);
#pragma unroll
  for (int i = 0; i < 11; ++i) st1(1, i);
  if (!KV) {
    asm volatile("s_waitcnt vmcnt(22)"
                 : "+v"(gx[0]), "+v"(gx[1]), "+v"(gx[2]), "+v"(gx[3]), "+v"(gx[4]),
                   "+v"(gwv[0]), "+v"(gwv[1]), "+v"(gwv[2]), "+v"(gwv[3]), "+v"(gwv[4]) :: "memory");
    typedef __bf16 bf2 __attribute__((ext_vector_type(2)));
    float t0 = 0.f, t1 = 0.f;
#pragma unroll
    for (int i = 0; i < 5; ++i)
#pragma unroll
      for (int e = 0; e < 4; e += 2) {
        const unsigned xa = gx[i][e], wa = gwv[i][e], xb = gx[i][e + 1], wb = gwv[i][e + 1];
        t0 = __builtin_amdgcn_fdot2_f32_bf16(__builtin_bit_cast(bf2, xa), __builtin_bit_cast(bf2, wa), t0, false);
        t1 = __builtin_amdgcn_fdot2_f32_bf16(__builtin_bit_cast(bf2, xb), __builtin_bit_cast(bf2, wb), t1, false);
      }
    float t = t0 + t1;
    t += __shfl_xor(t, 1); t += __shfl_xor(t, 2); t += __shfl_xor(t, 4); t += __shfl_xor(t, 8);
    ((float*)(ws + OFF_G))[grow] = 1.0f / (1.0f + __expf(-t));
  }
  auto extras = [&]() {
    const u16* bsrc = b1 + (KV ? 32 * (40 + j) : 160 * j) + min(lane, KV ? 3 : 19) * 8;
    __builtin_amdgcn_global_load_lds((const unsigned*)bsrc, (unsigned*)(smem + K1_LDS + 1024 + w * 1024), 16, 0, 0);
    const int gwv = min(blockIdx.x * 4 + w, 959), kb = gwv / 20, np = gwv % 20;
#pragma unroll
    for (int i = 0; i < 2; ++i)
      __builtin_amdgcn_global_load_lds((const unsigned*)(w2 + (size_t)(kb * 32 + i * 16 + (lane >> 2)) * DM + np * 32 + (lane & 3) * 8),
                                       (unsigned*)(smem + K1_LDS + 6144 + (w * 2 + i) * 1024), 16, 0, 0);
  };
  asm volatile("" ::: "memory");

  const int aoff0 = (wm * 64 + c) * 128 + ((g ^ (c & 7)) * 16);
  const int aoff1 = (wm * 64 + c) * 128 + (((g + 4) ^ (c & 7)) * 16);
  const int q4 = c >> 2, p4 = c & 3;
  int boff[7];
#pragma unroll
  for (int ni = 0; ni < 7; ++ni) {
    const int pos = slot_pos<KV>(wn, ni);
    boff[ni] = 16384 + (g >> 1) * 7168 + (8 * (g & 1) + q4) * 64 + (p4 & 1) * 8 +
               (((2 * (pos & 1) + (p4 >> 1)) ^ ((g & 1) << 1)) * 16) + (pos >> 1) * 1024;
  }

  f32x4 acc[4][7];
  struct Frags { bf16x8 a[4]; s16x4 bl[7], bh[7]; };
  Frags F0, F1;
  auto rd1 = [&](Frags& F, int buf, int kk, int idx) {
    const char* base = smem + buf * K1_STAGE;
    if (idx < 4) F.a[idx] = *(const bf16x8*)(base + (kk ? aoff1 : aoff0) + idx * 2048);
    else {
      const int ni = (idx - 4) >> 1, hi = (idx - 4) & 1;
      const char* bp = base + boff[ni] + kk * 14336 + hi * 256;
      s16x4 v = __builtin_amdgcn_ds_read_tr16_b64_v4i16((lds_s16x4_ptr)(bp));
      if (hi) F.bh[ni] = v; else F.bl[ni] = v;
    }
  };
  auto rdst = [&](Frags& F, int buf, int kk, int nb, int p0, int np) {
#pragma unroll
    for (int i = 0; i < 6; ++i) {
      rd1(F, buf, kk, 3 * i); rd1(F, buf, kk, 3 * i + 1); rd1(F, buf, kk, 3 * i + 2);
      if (i < np) st1(nb, p0 + i);
    }
  };
  auto mma = [&](Frags& F) {
#pragma unroll
    for (int ni = 0; ni < 7; ++ni) {
      bf16x8 fb = __builtin_shufflevector(F.bl[ni], F.bh[ni], 0, 1, 2, 3, 4, 5, 6, 7);
#pragma unroll
      for (int mi = 0; mi < 4; ++mi) {
        if (KV && ni < 5) acc[mi][ni] = MFMA16(F.a[mi], fb, acc[mi][ni]);
        else acc[mi][ni] = MFMA16(fb, F.a[mi], acc[mi][ni]);
      }
    }
  };

#define K1_INTERLEAVE(NV)                                                   \
  _Pragma("unroll") for (int i_ = 0; i_ < 6; ++i_) {                        \
    __builtin_amdgcn_sched_group_barrier(0x008, 1, 0);                      \
    __builtin_amdgcn_sched_group_barrier(0x100, 1, 0);                      \
    __builtin_amdgcn_sched_group_barrier(0x008, 1, 0);                      \
    __builtin_amdgcn_sched_group_barrier(0x100, 1, 0);                      \
    __builtin_amdgcn_sched_group_barrier(0x008, 1, 0);                      \
    __builtin_amdgcn_sched_group_barrier(0x100, 1, 0);                      \
    if (i_ < (NV)) __builtin_amdgcn_sched_group_barrier(0x010, 1, 0);       \
    __builtin_amdgcn_sched_group_barrier(0x008, 1, 0);                      \
  }                                                                         \
  __builtin_amdgcn_sched_group_barrier(0x008, 4, 0);

  const __amdgpu_buffer_rsrc_t rsO = __builtin_amdgcn_make_buffer_rsrc(ws, 0, (int)OFF_FLAG, 0x00020000);
  if (KV) asm volatile("s_waitcnt vmcnt(11)" ::: "memory");
  else asm volatile("s_waitcnt vmcnt(12)" ::: "memory");
  __builtin_amdgcn_s_barrier();
  asm volatile("" ::: "memory");
  STAMP(0, 1);
#pragma unroll
  for (int ni = 0; ni < 7; ++ni) {
    float iv;
    if (KV) iv = ni < 2 ? 1e-6f : (ni == 2 ? (wn ? 0.f : 1e-6f) : (ni == 5 ? (wn ? 1e-6f : 0.f) : 0.f));
    else iv = ni < 2 ? 1e-6f : 0.f;
#pragma unroll
    for (int mi = 0; mi < 4; ++mi) acc[mi][ni] = (f32x4){iv, iv, iv, iv};
  }
  rdst(F0, 0, 0, 2, 0, 6);
  __builtin_amdgcn_sched_barrier(0);
  rdst(F1, 0, 1, 2, 6, 5);
  mma(F0);
  K1_INTERLEAVE(5)
  __builtin_amdgcn_sched_barrier(0);
  extras();
  __builtin_amdgcn_sched_barrier(0);
  int buf = 1, nbuf = 0;
#pragma unroll 1
  for (int s = 1; s < 8; ++s) {
    if (s < 3) asm volatile("s_waitcnt lgkmcnt(0)\n\ts_waitcnt vmcnt(14)" ::: "memory");
    else asm volatile("s_waitcnt lgkmcnt(0)\n\ts_waitcnt vmcnt(11)" ::: "memory");
    LSTAMP(2 * s);
    __builtin_amdgcn_s_barrier();
    asm volatile("" ::: "memory");
    LSTAMP(2 * s + 1);
    __builtin_amdgcn_sched_barrier(0);
    rdst(F0, buf, 0, nbuf, 0, 6);
    mma(F1);
    K1_INTERLEAVE(6)
    __builtin_amdgcn_sched_barrier(0);
    rdst(F1, buf, 1, nbuf, 6, 5);
    mma(F0);
    K1_INTERLEAVE(5)
    __builtin_amdgcn_sched_barrier(0);
    nbuf = buf;
    buf = buf == 2 ? 0 : buf + 1;
  }
  STAMP(0, 2);
  asm volatile("s_waitcnt lgkmcnt(0)\n\ts_waitcnt vmcnt(11)" ::: "memory");
  __builtin_amdgcn_s_barrier();
  asm volatile("" ::: "memory");
  __builtin_amdgcn_sched_barrier(0);
  {
    const int gwv = min(blockIdx.x * 4 + w, 959);
#pragma unroll
    for (int i = 0; i < 2; ++i) {
      const char* bp = smem + K1_LDS + 6144 + w * 2048 + (4 * g + q4) * 64 + i * 32 + p4 * 8;
      s16x4 lo = __builtin_amdgcn_ds_read_tr16_b64_v4i16((lds_s16x4_ptr)(bp));
      s16x4 hi = __builtin_amdgcn_ds_read_tr16_b64_v4i16((lds_s16x4_ptr)(bp + 1024));
      bf16x8 v = __builtin_shufflevector(lo, hi, 0, 1, 2, 3, 4, 5, 6, 7);
      __builtin_amdgcn_raw_buffer_store_b128(__builtin_bit_cast(u32x4, v), rsO, lane * 16 + (int)OFF_W2F + (gwv * 2 + i) * 1024, 0, 16);
    }
    if ((blockIdx.x >> 3) < 5) {
      const int t = ((blockIdx.x >> 3) * 4 + w) * 64 + lane;
      __builtin_amdgcn_global_load_lds((const unsigned*)(wop + (size_t)(t >> 1) * (DM * 2) + (blockIdx.x & 7) * 160 + (t & 1) * 156),
                                       (unsigned*)(smem + K1_LDS), 4, 0, 0);
    }
  }
  __builtin_amdgcn_sched_barrier(0);
  rdst(F0, 2, 0, 0, 0, 0);
  mma(F1);
  K1_INTERLEAVE(0)
  __builtin_amdgcn_sched_barrier(0);
  rdst(F1, 2, 1, 0, 0, 0);
  mma(F0);
  K1_INTERLEAVE(0)
  __builtin_amdgcn_sched_barrier(0);
  asm volatile("s_waitcnt lgkmcnt(0)\n\ts_waitcnt vmcnt(2)" ::: "memory");
  __builtin_amdgcn_s_barrier();
  asm volatile("" ::: "memory");
  __builtin_amdgcn_sched_barrier(0);
  rdst(F0, 0, 0, 0, 0, 0);
  mma(F1);
  K1_INTERLEAVE(0)
  __builtin_amdgcn_sched_barrier(0);
  rdst(F1, 0, 1, 0, 0, 0);
  mma(F0);
  K1_INTERLEAVE(0)
  __builtin_amdgcn_sched_barrier(0);
  mma(F1);
  STAMP(0, 3);

  STAMP(0, 6);
  const int rt0 = rb * 8 + wm * 4;
#define K1AUX 16
  const int oHF = (int)OFF_HF + rt0 * 48 * 1024, oQF = (int)OFF_QF + rt0 * 20 * 1024;
  auto st16 = [&](int off, u32x4 v) { __builtin_amdgcn_raw_buffer_store_b128(v, rsO, lane * 16 + off, 0, K1AUX); };
  auto st8 = [&](int off, u32x2 v) { __builtin_amdgcn_raw_buffer_store_b64(v, rsO, lane * 16, off, 0); };
  u32x2 bias[7];
  {
    const char* bl = smem + K1_LDS + 1024 + w * 1024 + g * 8;
#pragma unroll
    for (int ni = 0; ni < 7; ++ni) {
      if (KV) { if (ni >= 5) bias[ni] = *(const u32x2*)(bl + 32 * (ni - 5)); }
      else if (ni >= 2) bias[ni] = *(const u32x2*)(bl + (ni < 6 ? 128 * wn + 32 * (ni - 2) : 256 + 32 * wn));
    }
  }
  auto addb = [&](const f32x4& a, const u32x2& b) -> f32x4 {
    return a + (f32x4){bf_lo(b[0]), bf_hi(b[0]), bf_lo(b[1]), bf_hi(b[1])};
  };

  if (!KV) {
#pragma unroll
    for (int mi = 0; mi < 4; ++mi) {
      u32x2 lo = cvt_q(acc[mi][0]), hi = cvt_q(acc[mi][1]);
      st16(oQF + (mi * 20 + 2 * j + wn) * 1024, (u32x4){lo[0], lo[1], hi[0], hi[1]});
    }
    STAMP(0, 7);
#pragma unroll
    for (int pr = 0; pr < 2; ++pr)
#pragma unroll
      for (int mi = 0; mi < 4; ++mi) {
        u32x2 lo = cvt_h(addb(acc[mi][2 + 2 * pr], bias[2 + 2 * pr])), hi = cvt_h(addb(acc[mi][3 + 2 * pr], bias[3 + 2 * pr]));
        st16(oHF + (mi * 48 + 5 * j + 2 * wn + pr) * 1024, (u32x4){lo[0], lo[1], hi[0], hi[1]});
      }
    STAMP(0, 8); STAMP(0, 9);
#pragma unroll
    for (int mi = 0; mi < 4; ++mi)
      st8(oHF + (mi * 48 + 5 * j + 4) * 1024 + wn * 8, cvt_h(addb(acc[mi][6], bias[6])));
  } else {
    asm volatile("s_waitcnt lgkmcnt(0)" ::: "memory");
    __builtin_amdgcn_s_barrier();
    asm volatile("" ::: "memory");
    STAMP(0, 7);
    char* xk = smem + (wm * 2) * 5 * 1024 + lane * 16;
    char* xv = xk + 20 * 1024;
    if (wn == 0) {
#pragma unroll
      for (int p = 0; p < 2; ++p) {
#pragma unroll
        for (int t = 0; t < 2; ++t) {
          u32x2 lo = cvt_v(acc[2 * p][3 + t]), hi = cvt_v(acc[2 * p + 1][3 + t]);
          *(u32x4*)(xv + (p * 5 + t) * 1024) = (u32x4){lo[0], lo[1], hi[0], hi[1]};
        }
#pragma unroll
        for (int t = 0; t < 3; ++t) {
          u32x2 lo = cvt_q(acc[2 * p][t]), hi = cvt_q(acc[2 * p + 1][t]);
          *(u32x4*)(xk + (p * 5 + t) * 1024) = (u32x4){lo[0], lo[1], hi[0], hi[1]};
        }
      }
    } else {
#pragma unroll
      for (int p = 0; p < 2; ++p) {
#pragma unroll
        for (int t = 0; t < 3; ++t) {
          u32x2 lo = cvt_v(acc[2 * p][2 + t]), hi = cvt_v(acc[2 * p + 1][2 + t]);
          *(u32x4*)(xv + (p * 5 + 2 + t) * 1024) = (u32x4){lo[0], lo[1], hi[0], hi[1]};
        }
#pragma unroll
        for (int t = 0; t < 2; ++t) {
          u32x2 lo = cvt_q(acc[2 * p][t]), hi = cvt_q(acc[2 * p + 1][t]);
          *(u32x4*)(xk + (p * 5 + 3 + t) * 1024) = (u32x4){lo[0], lo[1], hi[0], hi[1]};
        }
      }
    }
    asm volatile("s_waitcnt lgkmcnt(0)" ::: "memory");
    __builtin_amdgcn_s_barrier();
    asm volatile("" ::: "memory");
    STAMP(0, 8);
    const int a0 = w == 0 ? 0 : w - 1;
    bf16x8 pkA[4], vA3[3][4], pkB[4], vB[4];
#pragma unroll
    for (int ks4 = 0; ks4 < 4; ++ks4) {
      pkA[ks4] = *(const bf16x8*)(smem + (ks4 * 5 + w) * 1024 + lane * 16);
#pragma unroll
      for (int t = 0; t < 3; ++t) vA3[t][ks4] = *(const bf16x8*)(smem + (20 + ks4 * 5 + a0 + t) * 1024 + lane * 16);
    }
    const bool hasB = (w == 0 || w == 3);
    const int teB = w == 0 ? 4 : 3;
    if (hasB) {
#pragma unroll
      for (int ks4 = 0; ks4 < 4; ++ks4) {
        pkB[ks4] = *(const bf16x8*)(smem + (ks4 * 5 + 4) * 1024 + lane * 16);
        vB[ks4] = *(const bf16x8*)(smem + (20 + ks4 * 5 + teB) * 1024 + lane * 16);
      }
    }
    if (wn == 0) {
#pragma unroll
      for (int mi = 0; mi < 4; ++mi) {
        u32x2 lo = cvt_h(addb(acc[mi][5], bias[5])), hi = cvt_h(addb(acc[mi][6], bias[6]));
        st16(oHF + (mi * 48 + 40 + j) * 1024, (u32x4){lo[0], lo[1], hi[0], hi[1]});
      }
    } else {
#pragma unroll
      for (int mi = 0; mi < 4; ++mi)
        st8(oQF + (mi * 20 + 16 + (j >> 1)) * 1024 + (j & 1) * 8, cvt_q(acc[mi][5]));
    }
    STAMP(0, 9);
    u16* KVP = (u16*)(ws + OFF_KVP) + (size_t)j * 4 * 6400 + rb * 400;
    u16* ZP = (u16*)(ws + OFF_ZP) + j * 80 * 16 + rb;
    const bf16x8 ones = {0x3f80, 0x3f80, 0x3f80, 0x3f80, 0x3f80, 0x3f80, 0x3f80, 0x3f80};
    auto put_kv = [&](const f32x4& d, int te, int td) {
      const int dg = 16 * td + c, eg = 16 * te + 4 * g;
      const int hd = dg / 20, he = eg / 20;
      if (hd == he) *(u32x2*)(KVP + hd * 6400 + (dg - hd * 20) * 20 + (eg - he * 20)) = cvt_v(d);
    };
    auto put_z = [&](const f32x4& d, int td) {
      if (g == 0) ZP[(16 * td + c) * 16] = (u16)(pack2(d[0], 0.f) & 0xffffu);
    };
    f32x4 dA[4];
#pragma unroll
    for (int t = 0; t < 4; ++t) dA[t] = (f32x4){0.f, 0.f, 0.f, 0.f};
#pragma unroll
    for (int ks4 = 0; ks4 < 4; ++ks4) {
#pragma unroll
      for (int t = 0; t < 3; ++t) dA[t] = MFMA16(vA3[t][ks4], pkA[ks4], dA[t]);
      dA[3] = MFMA16(ones, pkA[ks4], dA[3]);
    }
#pragma unroll
    for (int t = 0; t < 3; ++t) put_kv(dA[t], a0 + t, w);
    put_z(dA[3], w);
    if (hasB) {
      f32x4 dB = {0.f, 0.f, 0.f, 0.f}, dZ = {0.f, 0.f, 0.f, 0.f};
#pragma unroll
      for (int ks4 = 0; ks4 < 4; ++ks4) { dB = MFMA16(vB[ks4], pkB[ks4], dB); dZ = MFMA16(ones, pkB[ks4], dZ); }
      put_kv(dB, teB, 4);
      if (w == 0) put_z(dZ, 4);
    }
  }
}

__global__ __launch_bounds__(256, 2) void k1_kernel(const u16* __restrict__ x, const u16* __restrict__ wqkv,
                                                   const u16* __restrict__ w1, const u16* __restrict__ b1,
                                                   const u16* __restrict__ w2, const u16* __restrict__ gate_w,
                                                   char* __restrict__ ws, const char* outp, const char* wop,
                                                   const char* b2p) {
  extern __shared__ __attribute__((aligned(16))) char smem[];
  const int bid = blockIdx.x;
  const int xcd = bid & 7, idx = bid >> 3;
  const int ri = idx & 7, ci = idx >> 3;
  const int rb = (xcd & 1) * 8 + ri;
  const int cq = (xcd >> 1) * 4 + ci;
  if (bid == 0 && threadIdx.x < 64) ((unsigned*)(ws + OFF_FLAG))[threadIdx.x * 64] = 0u;
  if (idx < 5 && threadIdx.x < 64) {
    const char* pc = (const char*)(__builtin_amdgcn_s_getpc() & ~255ull);
    __builtin_amdgcn_global_load_lds((const unsigned*)(pc + min(idx * 64 + (int)threadIdx.x, 289) * 128), (unsigned*)(smem + K1_LDS), 4, 0, 0);
  }
  if (idx >= 5 && idx < 10 && threadIdx.x < 64) {
    const int t = (idx - 5) * 64 + (int)threadIdx.x;
    constexpr int NWS = (int)((OFF_FLAG + 16384 + 65535) >> 16);
    const char* p = t < NWS ? ws + (size_t)t * 65536
                  : (t < NWS + 40 ? outp + (size_t)(t - NWS) * 65536
                  : (t < NWS + 53 ? wop + (size_t)(t - NWS - 40) * 65536 : b2p));
    __builtin_amdgcn_global_load_lds((const unsigned*)p, (unsigned*)(smem + K1_LDS), 4, 0, 0);
  }
  if (cq & 1) k1_body<false>(smem, rb, cq >> 1, ci, ri, x, wqkv, w1, b1, w2, gate_w, ws, wop);
  else k1_body<true>(smem, rb, cq >> 1, ci, ri, x, wqkv, w1, b1, w2, gate_w, ws, wop);
  STAMP(0, 4);
  asm volatile("s_waitcnt vmcnt(0)" ::: "memory");
  STAMP(0, 5); RSTAMP(0, 31);
}

typedef __attribute__((address_space(1))) unsigned gu32;
#define RLX_AGENT __ATOMIC_RELAXED, __HIP_MEMORY_SCOPE_AGENT
typedef __attribute__((address_space(3))) unsigned lu32;
DI void publish(char* ws, lu32* arrive, int u, int lane) {
  asm volatile("s_waitcnt vmcnt(0)" ::: "memory");
  if (lane == 0) {
    if (__hip_atomic_fetch_add(arrive, 1u, __ATOMIC_RELAXED, __HIP_MEMORY_SCOPE_WORKGROUP) == 1u) {
      __hip_atomic_fetch_add((gu32*)(ws + OFF_FLAG) + (u >> 3) * 64, 1u, RLX_AGENT);
      __hip_atomic_fetch_add((gu32*)(ws + OFF_FLAG) + (32 + (u & 7)) * 64, 1u, RLX_AGENT);
    }
  }
}
DI void task_w(int u, int lane, char* lds  , lu32* arrive, const u16* __restrict__ wo, char* ws) {
  const __amdgpu_buffer_rsrc_t rs = __builtin_amdgcn_make_buffer_rsrc(ws, 0, (int)OFF_FLAG, 0x00020000);
  const int h = u >> 3, cr = u & 7;
  const int g = lane >> 4, c = lane & 15;
  const u16* KVP = (const u16*)(ws + OFF_KVP) + h * 6400 + min(lane, 49) * 8;
  u32x4 kp[16];
#pragma unroll
  for (int p = 0; p < 16; ++p) kp[p] = *(const u32x4*)(KVP + p * 400);
#pragma unroll
  for (int i = 0; i < 4; ++i) {
    const int q = min(i * 64 + lane, 199), row = q / 10, ch = q - row * 10;
    __builtin_amdgcn_global_load_lds((const unsigned*)(wo + (size_t)(h * 20 + row) * DM + cr * 80 + ch * 8),
                                     (unsigned*)(lds + 2048 + i * 1024), 16, 0, 0);
  }
  __builtin_amdgcn_sched_barrier(0);
  __builtin_amdgcn_s_barrier();
  __builtin_amdgcn_sched_barrier(0);
  STAMP(2, 8);
  f32x4 s0 = {0.f, 0.f, 0.f, 0.f}, s1 = {0.f, 0.f, 0.f, 0.f};
#pragma unroll
  for (int p = 0; p < 16; ++p) {
    s0 += (f32x4){bf_lo(kp[p][0]), bf_hi(kp[p][0]), bf_lo(kp[p][1]), bf_hi(kp[p][1])};
    s1 += (f32x4){bf_lo(kp[p][2]), bf_hi(kp[p][2]), bf_lo(kp[p][3]), bf_hi(kp[p][3])};
  }
  asm volatile("s_waitcnt vmcnt(0)" ::: "memory");
  STAMP(2, 9);
  bf16x8 wf[5];
  {
    const int r0 = min(8 * g + (c >> 2), 19), r1 = min(8 * g + 4 + (c >> 2), 19);
#pragma unroll
    for (int t = 0; t < 5; ++t) {
      s16x4 lo = __builtin_amdgcn_ds_read_tr16_b64_v4i16((lds_s16x4_ptr)(lds + 2048 + r0 * 160 + t * 32 + (c & 3) * 8));
      s16x4 hi = __builtin_amdgcn_ds_read_tr16_b64_v4i16((lds_s16x4_ptr)(lds + 2048 + r1 * 160 + t * 32 + (c & 3) * 8));
      wf[t] = __builtin_shufflevector(lo, hi, 0, 1, 2, 3, 4, 5, 6, 7);
    }
  }
  *(u32x4*)(lds + lane * 32) = (u32x4){0u, 0u, 0u, 0u};
  *(u32x4*)(lds + lane * 32 + 16) = (u32x4){0u, 0u, 0u, 0u};
  __builtin_amdgcn_s_waitcnt(0xC07F);
  if (lane < 50) {
    const int i0 = lane * 8, d0 = i0 / 20, e0 = i0 - d0 * 20;
    *(u32x2*)(lds + d0 * 64 + e0 * 2) = (u32x2){pack2(s0[0], s0[1]), pack2(s0[2], s0[3])};
    const int i1 = i0 + 4, d1 = i1 / 20, e1 = i1 - d1 * 20;
    *(u32x2*)(lds + d1 * 64 + e1 * 2) = (u32x2){pack2(s1[0], s1[1]), pack2(s1[2], s1[3])};
  }
  __builtin_amdgcn_s_waitcnt(0xC07F);
  bf16x8 ka0 = *(const bf16x8*)(lds + c * 64 + g * 16);
  bf16x8 ka1 = *(const bf16x8*)(lds + (16 + c) * 64 + g * 16);
#pragma unroll
  for (int t = 0; t < 5; ++t) {
    f32x4 d0 = MFMA16(ka0, wf[t], ((f32x4){0.f, 0.f, 0.f, 0.f}));
    f32x4 d1 = MFMA16(ka1, wf[t], ((f32x4){0.f, 0.f, 0.f, 0.f}));
    {
      const int k = h * 20 + 4 * g;
      const int kb = k >> 5, half = (k >> 4) & 1, gg = (k & 15) >> 2;
      __builtin_amdgcn_raw_buffer_store_b64((u32x2){pack2(d0[0], d0[1]), pack2(d0[2], d0[3])}, rs,
          (int)OFF_WPF + (kb * 40 + cr * 5 + t) * 1024 + half * 512 + (gg * 16 + c) * 8, 0, 16);
    }
    if (g == 0) {
      const int k = h * 20 + 16;
      const int kb = k >> 5, half = (k >> 4) & 1, gg = (k & 15) >> 2;
      __builtin_amdgcn_raw_buffer_store_b64((u32x2){pack2(d1[0], d1[1]), pack2(d1[2], d1[3])}, rs,
          (int)OFF_WPF + (kb * 40 + cr * 5 + t) * 1024 + half * 512 + (gg * 16 + c) * 8, 0, 16);
    }
  }
  STAMP(2, 10);
  publish(ws, arrive, u, lane);
}

DI void task_a(int u, int lane, char* lds  , lu32* arrive, char* ws) {
  const __amdgpu_buffer_rsrc_t rs = __builtin_amdgcn_make_buffer_rsrc(ws, 0, (int)OFF_FLAG, 0x00020000);
  const int rt = u >> 1, kh = u & 1;
  const int g = lane >> 4, c = lane & 15;
  const u16* ZP = (const u16*)(ws + OFF_ZP) + kh * 320 * 16;
  u32x4 zp[10];
#pragma unroll
  for (int i = 0; i < 10; ++i) zp[i] = *(const u32x4*)(ZP + i * 512 + lane * 8);
  const char* QF = ws + OFF_QF + ((size_t)rt * 20 + kh * 10) * 1024;
  u32x4 qf[10];
#pragma unroll
  for (int b = 0; b < 10; ++b) qf[b] = *(const u32x4*)(QF + b * 1024 + lane * 16);
  const float gate = ((const float*)(ws + OFF_G))[rt * 16 + c];
  __builtin_amdgcn_sched_barrier(0);
  __builtin_amdgcn_s_barrier();
  __builtin_amdgcn_sched_barrier(0);
  STAMP(2, 8);
#pragma unroll
  for (int i = 0; i < 10; ++i) {
    float t = ((bf_lo(zp[i][0]) + bf_hi(zp[i][0])) + (bf_lo(zp[i][1]) + bf_hi(zp[i][1]))) +
              ((bf_lo(zp[i][2]) + bf_hi(zp[i][2])) + (bf_lo(zp[i][3]) + bf_hi(zp[i][3])));
    t += __builtin_bit_cast(float, __builtin_amdgcn_update_dpp(0, __builtin_bit_cast(int, t), 0xB1, 0xF, 0xF, true));
    if ((lane & 1) == 0) *(u16*)(lds + (i * 32 + (lane >> 1)) * 2) = (u16)(pack2(t, 0.f) & 0xffffu);
  }
  STAMP(2, 9);
  __builtin_amdgcn_s_waitcnt(0xC07F);
  f32x4 den = {0.f, 0.f, 0.f, 0.f};
#pragma unroll
  for (int b = 0; b < 10; ++b) {
    const int k0 = 32 * b + 4 * g, k1 = k0 + 16;
    u32x2 zl = *(const u32x2*)(lds + k0 * 2), zh = *(const u32x2*)(lds + k1 * 2);
    const bool m0 = (k0 / 20) == c, m1 = (k1 / 20) == c;
    u32x4 zf = {m0 ? zl[0] : 0u, m0 ? zl[1] : 0u, m1 ? zh[0] : 0u, m1 ? zh[1] : 0u};
    den = MFMA16(__builtin_bit_cast(bf16x8, zf), __builtin_bit_cast(bf16x8, qf[b]), den);
  }
  f32x4 rd;
#pragma unroll
  for (int r = 0; r < 4; ++r) rd[r] = gate / (den[r] + 1e-6f);
  *(f32x4*)(lds + 1024 + c * 64 + g * 16) = rd;
  __builtin_amdgcn_s_waitcnt(0xC07F);
#pragma unroll
  for (int b = 0; b < 10; ++b) {
    const int k0 = 32 * b + 4 * g, k1 = k0 + 16;
    const float r0 = *(const float*)(lds + 1024 + c * 64 + (k0 / 20) * 4);
    const float r1 = *(const float*)(lds + 1024 + c * 64 + (k1 / 20) * 4);
    u32x4 o;
    o[0] = pack2(bf_lo(qf[b][0]) * r0, bf_hi(qf[b][0]) * r0);
    o[1] = pack2(bf_lo(qf[b][1]) * r0, bf_hi(qf[b][1]) * r0);
    o[2] = pack2(bf_lo(qf[b][2]) * r1, bf_hi(qf[b][2]) * r1);
    o[3] = pack2(bf_lo(qf[b][3]) * r1, bf_hi(qf[b][3]) * r1);
    __builtin_amdgcn_raw_buffer_store_b128(o, rs, (int)OFF_AF + (rt * 20 + kh * 10 + b) * 1024 + lane * 16, 0, 16);
  }
  STAMP(2, 10);
  publish(ws, arrive, u, lane);
}

#define K2D 2
constexpr int K2_DEPTH = K2D;
__global__ __launch_bounds__(384, 2) void k2_kernel(const u16* __restrict__ wo, const u16* __restrict__ b2,
                                                   char* ws, u16* __restrict__ out) {
  __shared__ __attribute__((aligned(16))) char red[4 * 20 * 1024 + 8192 + 16];
  lu32* ready = (lu32*)(red + 81920 + 8192);
  const int bid = blockIdx.x;
  const int xcd = bid & 7, idx = bid >> 3;
  const int rb = (xcd >> 1) * 8 + (idx & 7);
  const int cb = (xcd & 1) * 4 + (idx >> 3);
  const int lane = threadIdx.x & 63;
  const int w = __builtin_amdgcn_readfirstlane(threadIdx.x >> 6);
  const int g = lane >> 4, c = lane & 15;

  if (w >= 4) {
    __builtin_amdgcn_s_setprio(3);
    RSTAMP(2, 30); STAMP(2, 0);
    if (w == 4) {
      if (lane < 3) __hip_atomic_store(ready + lane, 0u, __ATOMIC_RELAXED, __HIP_MEMORY_SCOPE_WORKGROUP);
      asm volatile("s_waitcnt lgkmcnt(0)" ::: "memory");
      task_w(bid, lane, red + 81920, ready + 2, wo, ws);
      STAMP(2, 1);
    } else {
      task_a(bid, lane, red + 81920 + 6144, ready + 2, ws);
      STAMP(2, 1);
      gu32* cntA = (gu32*)(ws + OFF_FLAG) + rb * 64;
      gu32* cntW = (gu32*)(ws + OFF_FLAG) + (32 + cb) * 64;
      for (unsigned spins = 0; spins < (1u << 24); ++spins) {
        if (__builtin_amdgcn_readfirstlane(__hip_atomic_load(ready + 1, __ATOMIC_RELAXED, __HIP_MEMORY_SCOPE_WORKGROUP)) != 0u) break;
        __builtin_amdgcn_s_sleep(2);
      }
      for (unsigned spins = 0; spins < (1u << 22); ++spins) {
        const unsigned a = __hip_atomic_load(cntA, RLX_AGENT), b = __hip_atomic_load(cntW, RLX_AGENT);
        if (__builtin_amdgcn_readfirstlane(a) == 8u && __builtin_amdgcn_readfirstlane(b) == 32u) break;
        __builtin_amdgcn_s_sleep(4);
      }
      if (lane == 0) __hip_atomic_store(ready, 1u, __ATOMIC_RELAXED, __HIP_MEMORY_SCOPE_WORKGROUP);
    }
    STAMP(2, 2); STAMP(2, 3); STAMP(2, 4); STAMP(2, 5); RSTAMP(2, 31);
    return;
  }

  const __amdgpu_buffer_rsrc_t rs = __builtin_amdgcn_make_buffer_rsrc(ws, 0, (int)OFF_FLAG, 0x00020000);
  const int voff = lane * 16;
  const int oH = (int)OFF_HF + (rb * 4 * 48 + w) * 1024;
  const int oA = (int)OFF_AF + (rb * 4 * 20 + w) * 1024;
  const int oW2 = (int)OFF_W2F + (w * 40 + cb * 5) * 1024;
  const int oWP = (int)OFF_WPF + (w * 40 + cb * 5) * 1024;

  RSTAMP(2, 30); STAMP(2, 0);
  f32x4 acc[4][5];
#pragma unroll
  for (int mi = 0; mi < 4; ++mi)
#pragma unroll
    for (int ni = 0; ni < 5; ++ni) acc[mi][ni] = (f32x4){0.f, 0.f, 0.f, 0.f};

  constexpr int NS = K2_DEPTH + 1;
  u32x4 fa[NS][4], fb[NS][5];
  float gv[4];
  u32x2 b2v[5];
#define K2ROTR 0
#define K2ROTC 0
  const int rot = (((idx & 7) * K2ROTR) / 2 + ((idx >> 3) * K2ROTC) / 2) % 12;
  auto load = [&](int i, int slot) {
    if (i < 12) {
      int ir = i + rot; ir = ir >= 12 ? ir - 12 : ir;
#pragma unroll
      for (int ni = 0; ni < 5; ++ni) fb[slot][ni] = __builtin_amdgcn_raw_buffer_load_b128(rs, voff, oW2 + (4 * ir * 40 + ni) * 1024, 0);
#pragma unroll
      for (int mi = 0; mi < 4; ++mi) fa[slot][mi] = __builtin_amdgcn_raw_buffer_load_b128(rs, voff, oH + (mi * 48 + 4 * ir) * 1024, 0);
    } else {
      const int ii = i - 12;
#pragma unroll
      for (int ni = 0; ni < 5; ++ni) {
        u32x2 lo = __builtin_amdgcn_raw_buffer_load_b64(rs, lane * 8, oWP + (4 * ii * 40 + ni) * 1024, 16);
        u32x2 hi = __builtin_amdgcn_raw_buffer_load_b64(rs, lane * 8, oWP + (4 * ii * 40 + ni) * 1024 + 512, 16);
        fb[slot][ni] = (u32x4){lo[0], lo[1], hi[0], hi[1]};
      }
#pragma unroll
      for (int mi = 0; mi < 4; ++mi) fa[slot][mi] = __builtin_amdgcn_raw_buffer_load_b128(rs, voff, oA + (mi * 20 + 4 * ii) * 1024, 16);
    }
  };
  __builtin_amdgcn_s_barrier();
  __builtin_amdgcn_sched_barrier(0);
#pragma unroll
  for (int d = 0; d < K2_DEPTH; ++d) load(d, d);
#pragma unroll
  for (int mi = 0; mi < 4; ++mi)
    gv[mi] = __uint_as_float(__builtin_amdgcn_raw_buffer_load_b32(rs, c * 4, (int)OFF_G + (rb * 64 + mi * 16) * 4, 0));
  __builtin_amdgcn_sched_barrier(0);
#pragma unroll
  for (int i = 0; i < 17; ++i) {
    const int slot = i % NS;
    if (i == 1) { STAMP(2, 1); }
#define K2GO 6
    if (i == K2GO) {
      if (lane == 0) __hip_atomic_store(ready + 1, 1u, __ATOMIC_RELAXED, __HIP_MEMORY_SCOPE_WORKGROUP);
      __builtin_amdgcn_sched_barrier(0);
    }
    if (i == 12 - K2_DEPTH) {
      STAMP(2, 6);
      for (unsigned spins = 0; spins < (1u << 24); ++spins) {
        if (__builtin_amdgcn_readfirstlane(__hip_atomic_load(ready, __ATOMIC_RELAXED, __HIP_MEMORY_SCOPE_WORKGROUP)) != 0u) break;
        __builtin_amdgcn_s_sleep(1);
      }
      asm volatile("" ::: "memory");
      STAMP(2, 7);
      __builtin_amdgcn_sched_barrier(0);
    }
    if (i == 12) {
      STAMP(2, 2);
#pragma unroll
      for (int mi = 0; mi < 4; ++mi) {
        gv[mi] = 1.0f - gv[mi];
#pragma unroll
        for (int ni = 0; ni < 5; ++ni) acc[mi][ni] *= gv[mi];
      }
      __builtin_amdgcn_sched_barrier(0);
    }
    if (i + K2_DEPTH < 17) load(i + K2_DEPTH, (i + K2_DEPTH) % NS);
    if (i == 15) {
#pragma unroll
      for (int ni = 0; ni < 5; ++ni) b2v[ni] = *(const u32x2*)(b2 + cb * 80 + ni * 16 + 4 * g);
      __builtin_amdgcn_sched_barrier(0);
    }
#pragma unroll
    for (int mi = 0; mi < 4; ++mi)
#pragma unroll
      for (int ni = 0; ni < 5; ++ni)
        acc[mi][ni] = MFMA16(__builtin_bit_cast(bf16x8, fb[slot][ni]), __builtin_bit_cast(bf16x8, fa[slot][mi]), acc[mi][ni]);
    if (i + K2_DEPTH < 12) {
#pragma unroll
      for (int k = 0; k < 9; ++k) {
        __builtin_amdgcn_sched_group_barrier(0x008, 2, 0);
        __builtin_amdgcn_sched_group_barrier(0x020, 1, 0);
      }
      __builtin_amdgcn_sched_group_barrier(0x008, 2, 0);
    } else if (i + K2_DEPTH < 17) {
#pragma unroll
      for (int k = 0; k < 14; ++k) {
        __builtin_amdgcn_sched_group_barrier(0x008, 1, 0);
        __builtin_amdgcn_sched_group_barrier(0x020, 1, 0);
      }
      __builtin_amdgcn_sched_group_barrier(0x008, 6, 0);
    }
    __builtin_amdgcn_sched_barrier(0);
  }

  STAMP(2, 3);
#pragma unroll
  for (int mi = 0; mi < 4; ++mi)
    if (mi != w) {
#pragma unroll
      for (int ni = 0; ni < 5; ++ni) *(f32x4*)(red + ((w * 4 + mi) * 5 + ni) * 1024 + lane * 16) = acc[mi][ni];
    }
  __syncthreads();
  float gw = gv[0];
  f32x4 mine[5];
#pragma unroll
  for (int ni = 0; ni < 5; ++ni) mine[ni] = acc[0][ni];
#pragma unroll
  for (int mi = 1; mi < 4; ++mi)
    if (w == mi) {
      gw = gv[mi];
#pragma unroll
      for (int ni = 0; ni < 5; ++ni) mine[ni] = acc[mi][ni];
    }
#pragma unroll
  for (int ni = 0; ni < 5; ++ni) {
    f32x4 s = mine[ni];
#pragma unroll
    for (int o = 1; o < 4; ++o) {
      const int ow = (w + o) & 3;
      s += *(const f32x4*)(red + ((ow * 4 + w) * 5 + ni) * 1024 + lane * 16);
    }
    const int n = cb * 80 + ni * 16 + 4 * g;
    const u32x2 bv = b2v[ni];
    s[0] += gw * bf_lo(bv[0]); s[1] += gw * bf_hi(bv[0]); s[2] += gw * bf_lo(bv[1]); s[3] += gw * bf_hi(bv[1]);
    *(u32x2*)(out + (size_t)(rb * 64 + w * 16 + c) * DM + n) = (u32x2){pack2(s[0], s[1]), pack2(s[2], s[3])};
  }
  STAMP(2, 4);
  STAMP(2, 5); RSTAMP(2, 31);
}

extern "C" void kernel_launch(void* const* d_in, const int* in_sizes, int n_in, void* d_out, int out_size,
                              void* d_ws, size_t ws_size, hipStream_t stream) {
  const u16* x = (const u16*)d_in[0];
  const u16* wqkv = (const u16*)d_in[1];
  const u16* wo = (const u16*)d_in[2];
  const u16* w1 = (const u16*)d_in[4];
  const u16* b1 = (const u16*)d_in[5];
  const u16* w2 = (const u16*)d_in[6];
  const u16* b2 = (const u16*)d_in[7];
  const u16* gw = (const u16*)d_in[8];
  char* ws = (char*)d_ws;
  (void)hipFuncSetAttribute((const void*)k1_kernel, hipFuncAttributeMaxDynamicSharedMemorySize, K1_LDS + 14336);
  k1_kernel<<<256, 256, K1_LDS + 14336, stream>>>(x, wqkv, w1, b1, w2, gw, ws, (const char*)d_out, (const char*)wo, (const char*)b2);
  k2_kernel<<<256, 384, 0, stream>>>(wo, b2, ws, (u16*)d_out);
}
```

```cpp
#include <hip/hip_runtime.h>
#include <hip/hip_bf16.h>

typedef unsigned short u16;
typedef short bf16x8 __attribute__((ext_vector_type(8)));
typedef short s16x4 __attribute__((ext_vector_type(4)));
typedef float f32x4 __attribute__((ext_vector_type(4)));
typedef unsigned u32x4 __attribute__((ext_vector_type(4)));
typedef unsigned u32x2 __attribute__((ext_vector_type(2)));
typedef __attribute__((address_space(3))) s16x4* lds_s16x4_ptr;
#define DI __device__ __forceinline__
#define MFMA16(a, b, c) __builtin_amdgcn_mfma_f32_16x16x32_bf16((a), (b), (c), 0, 0, 0)

constexpr int SEQ = 2048, DM = 640, FF = 1536, NQKV = 1920;

constexpr size_t OFF_HF = 0;
constexpr size_t OFF_QF = OFF_HF + 128ull * 48 * 1024;
constexpr size_t OFF_AF = OFF_QF + 128ull * 20 * 1024;
constexpr size_t OFF_W2F = OFF_AF + 128ull * 20 * 1024;
constexpr size_t OFF_WPF = OFF_W2F + 48ull * 40 * 1024;
constexpr size_t OFF_KVP = OFF_WPF + 20ull * 40 * 1024;
constexpr size_t OFF_ZP = OFF_KVP + 16ull * 32 * 1600;
constexpr size_t OFF_G = OFF_ZP + 16ull * 640 * 4;
constexpr size_t OFF_FLAG = OFF_G + 2048 * 4;
constexpr size_t OFF_DBG = 64ull << 20;
#define STAMP(kern, idx)
#define RSTAMP(kern, idx)
#define LSTAMP(idx)

DI unsigned pack2(float lo, float hi) {
  typedef __bf16 bf2 __attribute__((ext_vector_type(2)));
  typedef float f2 __attribute__((ext_vector_type(2)));
  f2 v = {lo, hi};
  bf2 r = __builtin_convertvector(v, bf2);
  return __builtin_bit_cast(unsigned, r);
}
DI float bf_lo(unsigned u) { return __uint_as_float(u << 16); }
DI float bf_hi(unsigned u) { return __uint_as_float(u & 0xffff0000u); }
DI float rbf(float f) { return bf_lo(pack2(f, 0.f) & 0xffffu); }
DI float phi(float t) {
  float e = __builtin_amdgcn_exp2f(t * 1.4426950408889634f);
  e = fminf(e, 1.0f);
  return fmaxf(t, 0.f) + e + 1e-6f;
}

constexpr int K1_STAGE = 45056;
constexpr int K1_LDS = 3 * K1_STAGE;

struct ColSrc { const u16* p; int stride; };
template <bool KV> DI ColSrc pos_src(int pos, int j, const u16* wqkv, const u16* w1) {
  ColSrc s;
  if (KV) {
    if (pos < 5) { s.p = wqkv + 640 + 80 * j + 16 * pos; s.stride = NQKV; }
    else if (pos == 5) { s.p = wqkv + 16 * (32 + j); s.stride = NQKV; }
    else if (pos < 11) { s.p = wqkv + 1280 + 80 * j + 16 * (pos - 6); s.stride = NQKV; }
    else if (pos == 11) { s.p = wqkv + 1280 + 80 * j + 64; s.stride = NQKV; }
    else { s.p = w1 + 32 * (40 + j) + 16 * (pos - 12); s.stride = FF; }
  } else {
    if (pos < 2) { s.p = wqkv + 32 * (2 * j) + 16 * pos; s.stride = NQKV; }
    else if (pos < 6) { s.p = w1 + 32 * (5 * j) + 16 * (pos - 2); s.stride = FF; }
    else if (pos < 8) { s.p = wqkv + 32 * (2 * j + 1) + 16 * (pos - 6); s.stride = NQKV; }
    else { s.p = w1 + 32 * (5 * j + 2) + 16 * (pos - 8); s.stride = FF; }
  }
  return s;
}
template <bool KV> DI int slot_pos(int wn, int ni) {
  if (KV) {
    const int p0 = ni < 3 ? ni : (ni < 5 ? ni + 3 : ni + 7);
    const int p1 = ni < 2 ? ni + 3 : (ni < 5 ? ni + 6 : (ni == 5 ? 5 : 11));
    return wn ? p1 : p0;
  }
  return ni < 6 ? 6 * wn + ni : 12 + wn;
}
DI float relu_f(float v) { return __int_as_float(max(__float_as_int(v), 0)); }
DI float phi1(float t1) {
  float e = __builtin_amdgcn_exp2f(__builtin_fmaf(t1, 1.4426950408889634f, -1.4426950408889634e-6f));
  e = __builtin_amdgcn_fmed3f(e, 0.0f, 1.0f);
  return __int_as_float(max(__float_as_int(t1), __float_as_int(1e-6f))) + e;
}
DI u32x2 cvt_q(const f32x4& a) { return (u32x2){pack2(phi1(a[0]), phi1(a[1])), pack2(phi1(a[2]), phi1(a[3]))}; }
DI u32x2 cvt_h(const f32x4& a) {
  float t0 = relu_f(a[0]), t1 = relu_f(a[1]), t2 = relu_f(a[2]), t3 = relu_f(a[3]);
  return (u32x2){pack2(t0 * t0, t1 * t1), pack2(t2 * t2, t3 * t3)};
}
DI u32x2 cvt_v(const f32x4& a) { return (u32x2){pack2(a[0], a[1]), pack2(a[2], a[3])}; }

template <bool KV>
DI void k1_body(char* smem, int rb, int j, int ci, int ri, const u16* __restrict__ x, const u16* __restrict__ wqkv,
                const u16* __restrict__ w1, const u16* __restrict__ b1, const u16* __restrict__ w2,
                const u16* __restrict__ gate_w, char* __restrict__ ws) {
  const int tid = threadIdx.x;
  const int lane = tid & 63;
  const int w = __builtin_amdgcn_readfirstlane(tid >> 6);
  const int wm = w >> 1, wn = w & 1;
  const int g = lane >> 4, c = lane & 15;
  RSTAMP(0, 30); STAMP(0, 0);

  const __amdgpu_buffer_rsrc_t rsX = __builtin_amdgcn_make_buffer_rsrc((void*)x, 0, SEQ * DM * 2, 0x00020000);
  const __amdgpu_buffer_rsrc_t rsQ = __builtin_amdgcn_make_buffer_rsrc((void*)wqkv, 0, DM * NQKV * 2, 0x00020000);
  const __amdgpu_buffer_rsrc_t rsW = __builtin_amdgcn_make_buffer_rsrc((void*)w1, 0, DM * FF * 2, 0x00020000);
  const int vA = (lane >> 3) * (DM * 2) + (((lane & 7) ^ ((lane >> 3) & 7)) * 16);
#define NCOM 2
  const int rot = NCOM ? (ri + 2 * ci) % (10 - NCOM) : (ri + (5 * ci) / 2) % 10;
  int ks = NCOM ? 0 : rot, cnt = 0;
  int sA = (rb * 128 + 32 * w) * (DM * 2) + ks * 128;
  const int r16 = lane >> 2;
  const int cs = (lane & 3) ^ (((r16 >> 3) & 1) << 1);
  const int vQ = r16 * (NQKV * 2) + cs * 16;
  const int vW = r16 * (FF * 2) + cs * 16;
  const int vS = r16 * (NQKV * 2) + (cs & 1) * 16 + ((cs >> 1) ? 0 : ((640 + 80 * j + 64) - 16 * (32 + j)) * 2);
  const int vU = r16 * (NQKV * 2) + (cs & 1) * 16;
  int sQ = (w * 16 + ks * 64) * (NQKV * 2), sW = (w * 16 + ks * 64) * (FF * 2);
  auto st1 = [&](int buf, int i) {
    char* base = smem + buf * K1_STAGE;
    typedef __attribute__((address_space(3))) void* ldsp;
    if (i < 4) {
      __builtin_amdgcn_raw_ptr_buffer_load_lds(rsX, (ldsp)(base + (w * 4 + i) * 1024), 16, vA, sA + i * 8 * DM * 2, 0, 0);

    } else {
      const int p = i - 4;
      ldsp dst = (ldsp)(base + 16384 + (w * 7 + p) * 1024);
      const int col = (int)(pos_src<KV>((KV && p == 2) ? 5 : 2 * p, j, (const u16*)0, (const u16*)0).p - (const u16*)0) * 2;
      const bool isW = KV ? (p == 6) : !(p == 0 || p == 3);
      if (isW) __builtin_amdgcn_raw_ptr_buffer_load_lds(rsW, dst, 16, vW, sW + col, 0, 0);
      else __builtin_amdgcn_raw_ptr_buffer_load_lds(rsQ, dst, 16, (KV && p == 2) ? vS : ((KV && p == 5) ? vU : vQ), sQ + col, 0, 0);
      if (p == 6) {
        ++cnt;
        const int adv = cnt == NCOM ? 1 + rot : (ks == 9 ? -(9 - NCOM) : 1);
        ks += adv; sA += adv * 128; sQ += adv * (64 * NQKV * 2); sW += adv * (64 * FF * 2);
      }
    }
  };
  asm volatile("" ::: "memory");
  const int grow = rb * 128 + j * 16 + w * 4 + g;
  u32x4 gx[5], gwv[5];
  if (!KV) {
    const u16* xp = x + (size_t)grow * DM + c * 8;
    const u16* gp = gate_w + c * 8;
#pragma unroll
    for (int i = 0; i < 5; ++i) {
      asm volatile("global_load_dwordx4 %0, %1, off offset:%2" : "=&v"(gx[i]) : "v"(xp), "n"(i * 256) : "memory");
      asm volatile("global_load_dwordx4 %0, %1, off offset:%2" : "=&v"(gwv[i]) : "v"(gp), "n"(i * 256) : "memory");
    }
  }
#pragma unroll
  for (int i = 0; i < 11; ++i) st1(0, i);
#pragma unroll
  for (int i = 0; i < 11; ++i) st1(1, i);
  if (!KV) {
    asm volatile("s_waitcnt vmcnt(22)"
                 : "+v"(gx[0]), "+v"(gx[1]), "+v"(gx[2]), "+v"(gx[3]), "+v"(gx[4]),
                   "+v"(gwv[0]), "+v"(gwv[1]), "+v"(gwv[2]), "+v"(gwv[3]), "+v"(gwv[4]) :: "memory");
    typedef __bf16 bf2 __attribute__((ext_vector_type(2)));
    float t0 = 0.f, t1 = 0.f;
#pragma unroll
    for (int i = 0; i < 5; ++i)
#pragma unroll
      for (int e = 0; e < 4; e += 2) {
        const unsigned xa = gx[i][e], wa = gwv[i][e], xb = gx[i][e + 1], wb = gwv[i][e + 1];
        t0 = __builtin_amdgcn_fdot2_f32_bf16(__builtin_bit_cast(bf2, xa), __builtin_bit_cast(bf2, wa), t0, false);
        t1 = __builtin_amdgcn_fdot2_f32_bf16(__builtin_bit_cast(bf2, xb), __builtin_bit_cast(bf2, wb), t1, false);
      }
    float t = t0 + t1;
    t += __shfl_xor(t, 1); t += __shfl_xor(t, 2); t += __shfl_xor(t, 4); t += __shfl_xor(t, 8);
    ((float*)(ws + OFF_G))[grow] = 1.0f / (1.0f + __expf(-t));
  }
  auto extras = [&]() {
    const u16* bsrc = b1 + (KV ? 32 * (40 + j) : 160 * j) + min(lane, KV ? 3 : 19) * 8;
    __builtin_amdgcn_global_load_lds((const unsigned*)bsrc, (unsigned*)(smem + K1_LDS + 1024 + w * 1024), 16, 0, 0);
    const int gwv = min(blockIdx.x * 4 + w, 959), kb = gwv / 20, np = gwv % 20;
#pragma unroll
    for (int i = 0; i < 2; ++i)
      __builtin_amdgcn_global_load_lds((const unsigned*)(w2 + (size_t)(kb * 32 + i * 16 + (lane >> 2)) * DM + np * 32 + (lane & 3) * 8),
                                       (unsigned*)(smem + K1_LDS + 6144 + (w * 2 + i) * 1024), 16, 0, 0);
  };
  asm volatile("" ::: "memory");

  const int aoff0 = (wm * 64 + c) * 128 + ((g ^ (c & 7)) * 16);
  const int aoff1 = (wm * 64 + c) * 128 + (((g + 4) ^ (c & 7)) * 16);
  const int q4 = c >> 2, p4 = c & 3;
  int boff[7];
#pragma unroll
  for (int ni = 0; ni < 7; ++ni) {
    const int pos = slot_pos<KV>(wn, ni);
    boff[ni] = 16384 + (g >> 1) * 7168 + (8 * (g & 1) + q4) * 64 + (p4 & 1) * 8 +
               (((2 * (pos & 1) + (p4 >> 1)) ^ ((g & 1) << 1)) * 16) + (pos >> 1) * 1024;
  }

  f32x4 acc[4][7];
  struct Frags { bf16x8 a[4]; s16x4 bl[7], bh[7]; };
  Frags F0, F1;
  auto rd1 = [&](Frags& F, int buf, int kk, int idx) {
    const char* base = smem + buf * K1_STAGE;
    if (idx < 4) F.a[idx] = *(const bf16x8*)(base + (kk ? aoff1 : aoff0) + idx * 2048);
    else {
      const int ni = (idx - 4) >> 1, hi = (idx - 4) & 1;
      const char* bp = base + boff[ni] + kk * 14336 + hi * 256;
      s16x4 v = __builtin_amdgcn_ds_read_tr16_b64_v4i16((lds_s16x4_ptr)(bp));
      if (hi) F.bh[ni] = v; else F.bl[ni] = v;
    }
  };
  auto rdst = [&](Frags& F, int buf, int kk, int nb, int p0, int np) {
#pragma unroll
    for (int i = 0; i < 6; ++i) {
      rd1(F, buf, kk, 3 * i); rd1(F, buf, kk, 3 * i + 1); rd1(F, buf, kk, 3 * i + 2);
      if (i < np) st1(nb, p0 + i);
    }
  };
  auto mma = [&](Frags& F) {
#pragma unroll
    for (int ni = 0; ni < 7; ++ni) {
      bf16x8 fb = __builtin_shufflevector(F.bl[ni], F.bh[ni], 0, 1, 2, 3, 4, 5, 6, 7);
#pragma unroll
      for (int mi = 0; mi < 4; ++mi) {
        if (KV && ni < 5) acc[mi][ni] = MFMA16(F.a[mi], fb, acc[mi][ni]);
        else acc[mi][ni] = MFMA16(fb, F.a[mi], acc[mi][ni]);
      }
    }
  };

#define K1_INTERLEAVE(NV)                                                   \
  _Pragma("unroll") for (int i_ = 0; i_ < 6; ++i_) {                        \
    __builtin_amdgcn_sched_group_barrier(0x008, 1, 0);                      \
    __builtin_amdgcn_sched_group_barrier(0x100, 1, 0);                      \
    __builtin_amdgcn_sched_group_barrier(0x008, 1, 0);                      \
    __builtin_amdgcn_sched_group_barrier(0x100, 1, 0);                      \
    __builtin_amdgcn_sched_group_barrier(0x008, 1, 0);                      \
    __builtin_amdgcn_sched_group_barrier(0x100, 1, 0);                      \
    if (i_ < (NV)) __builtin_amdgcn_sched_group_barrier(0x010, 1, 0);       \
    __builtin_amdgcn_sched_group_barrier(0x008, 1, 0);                      \
  }                                                                         \
  __builtin_amdgcn_sched_group_barrier(0x008, 4, 0);

  const __amdgpu_buffer_rsrc_t rsO = __builtin_amdgcn_make_buffer_rsrc(ws, 0, (int)OFF_FLAG, 0x00020000);
  if (KV) asm volatile("s_waitcnt vmcnt(11)" ::: "memory");
  else asm volatile("s_waitcnt vmcnt(12)" ::: "memory");
  __builtin_amdgcn_s_barrier();
  asm volatile("" ::: "memory");
  STAMP(0, 1);
#pragma unroll
  for (int ni = 0; ni < 7; ++ni) {
    float iv;
    if (KV) iv = ni < 2 ? 1e-6f : (ni == 2 ? (wn ? 0.f : 1e-6f) : (ni == 5 ? (wn ? 1e-6f : 0.f) : 0.f));
    else iv = ni < 2 ? 1e-6f : 0.f;
#pragma unroll
    for (int mi = 0; mi < 4; ++mi) acc[mi][ni] = (f32x4){iv, iv, iv, iv};
  }
  rdst(F0, 0, 0, 2, 0, 6);
  __builtin_amdgcn_sched_barrier(0);
  rdst(F1, 0, 1, 2, 6, 5);
  mma(F0);
  K1_INTERLEAVE(5)
  __builtin_amdgcn_sched_barrier(0);
  extras();
  __builtin_amdgcn_sched_barrier(0);
  int buf = 1, nbuf = 0;
#pragma unroll 1
  for (int s = 1; s < 8; ++s) {
    if (s < 3) asm volatile("s_waitcnt lgkmcnt(0)\n\ts_waitcnt vmcnt(14)" ::: "memory");
    else asm volatile("s_waitcnt lgkmcnt(0)\n\ts_waitcnt vmcnt(11)" ::: "memory");
    LSTAMP(2 * s);
    __builtin_amdgcn_s_barrier();
    asm volatile("" ::: "memory");
    LSTAMP(2 * s + 1);
    __builtin_amdgcn_sched_barrier(0);
    rdst(F0, buf, 0, nbuf, 0, 6);
    mma(F1);
    K1_INTERLEAVE(6)
    __builtin_amdgcn_sched_barrier(0);
    rdst(F1, buf, 1, nbuf, 6, 5);
    mma(F0);
    K1_INTERLEAVE(5)
    __builtin_amdgcn_sched_barrier(0);
    nbuf = buf;
    buf = buf == 2 ? 0 : buf + 1;
  }
  STAMP(0, 2);
  asm volatile("s_waitcnt lgkmcnt(0)\n\ts_waitcnt vmcnt(11)" ::: "memory");
  __builtin_amdgcn_s_barrier();
  asm volatile("" ::: "memory");
  __builtin_amdgcn_sched_barrier(0);
  {
    const int gwv = min(blockIdx.x * 4 + w, 959);
#pragma unroll
    for (int i = 0; i < 2; ++i) {
      const char* bp = smem + K1_LDS + 6144 + w * 2048 + (4 * g + q4) * 64 + i * 32 + p4 * 8;
      s16x4 lo = __builtin_amdgcn_ds_read_tr16_b64_v4i16((lds_s16x4_ptr)(bp));
      s16x4 hi = __builtin_amdgcn_ds_read_tr16_b64_v4i16((lds_s16x4_ptr)(bp + 1024));
      bf16x8 v = __builtin_shufflevector(lo, hi, 0, 1, 2, 3, 4, 5, 6, 7);
      __builtin_amdgcn_raw_buffer_store_b128(__builtin_bit_cast(u32x4, v), rsO, lane * 16 + (int)OFF_W2F + (gwv * 2 + i) * 1024, 0, 16);
    }
  }
  __builtin_amdgcn_sched_barrier(0);
  rdst(F0, 2, 0, 0, 0, 0);
  mma(F1);
  K1_INTERLEAVE(0)
  __builtin_amdgcn_sched_barrier(0);
  rdst(F1, 2, 1, 0, 0, 0);
  mma(F0);
  K1_INTERLEAVE(0)
  __builtin_amdgcn_sched_barrier(0);
  asm volatile("s_waitcnt lgkmcnt(0)\n\ts_waitcnt vmcnt(2)" ::: "memory");
  __builtin_amdgcn_s_barrier();
  asm volatile("" ::: "memory");
  __builtin_amdgcn_sched_barrier(0);
  rdst(F0, 0, 0, 0, 0, 0);
  mma(F1);
  K1_INTERLEAVE(0)
  __builtin_amdgcn_sched_barrier(0);
  rdst(F1, 0, 1, 0, 0, 0);
  mma(F0);
  K1_INTERLEAVE(0)
  __builtin_amdgcn_sched_barrier(0);
  mma(F1);
  STAMP(0, 3);

  STAMP(0, 6);
  const int rt0 = rb * 8 + wm * 4;
#define K1AUX 16
  const int oHF = (int)OFF_HF + rt0 * 48 * 1024, oQF = (int)OFF_QF + rt0 * 20 * 1024;
  auto st16 = [&](int off, u32x4 v) { __builtin_amdgcn_raw_buffer_store_b128(v, rsO, lane * 16 + off, 0, K1AUX); };
  auto st8 = [&](int off, u32x2 v) { __builtin_amdgcn_raw_buffer_store_b64(v, rsO, lane * 16, off, 0); };
  u32x2 bias[7];
  {
    const char* bl = smem + K1_LDS + 1024 + w * 1024 + g * 8;
#pragma unroll
    for (int ni = 0; ni < 7; ++ni) {
      if (KV) { if (ni >= 5) bias[ni] = *(const u32x2*)(bl + 32 * (ni - 5)); }
      else if (ni >= 2) bias[ni] = *(const u32x2*)(bl + (ni < 6 ? 128 * wn + 32 * (ni - 2) : 256 + 32 * wn));
    }
  }
  auto addb = [&](const f32x4& a, const u32x2& b) -> f32x4 {
    return a + (f32x4){bf_lo(b[0]), bf_hi(b[0]), bf_lo(b[1]), bf_hi(b[1])};
  };

  if (!KV) {
#pragma unroll
    for (int mi = 0; mi < 4; ++mi) {
      u32x2 lo = cvt_q(acc[mi][0]), hi = cvt_q(acc[mi][1]);
      st16(oQF + (mi * 20 + 2 * j + wn) * 1024, (u32x4){lo[0], lo[1], hi[0], hi[1]});
    }
    STAMP(0, 7);
#pragma unroll
    for (int pr = 0; pr < 2; ++pr)
#pragma unroll
      for (int mi = 0; mi < 4; ++mi) {
        u32x2 lo = cvt_h(addb(acc[mi][2 + 2 * pr], bias[2 + 2 * pr])), hi = cvt_h(addb(acc[mi][3 + 2 * pr], bias[3 + 2 * pr]));
        st16(oHF + (mi * 48 + 5 * j + 2 * wn + pr) * 1024, (u32x4){lo[0], lo[1], hi[0], hi[1]});
      }
    STAMP(0, 8); STAMP(0, 9);
#pragma unroll
    for (int mi = 0; mi < 4; ++mi)
      st8(oHF + (mi * 48 + 5 * j + 4) * 1024 + wn * 8, cvt_h(addb(acc[mi][6], bias[6])));
  } else {
    asm volatile("s_waitcnt lgkmcnt(0)" ::: "memory");
    __builtin_amdgcn_s_barrier();
    asm volatile("" ::: "memory");
    STAMP(0, 7);
    char* xk = smem + (wm * 2) * 5 * 1024 + lane * 16;
    char* xv = xk + 20 * 1024;
    if (wn == 0) {
#pragma unroll
      for (int p = 0; p < 2; ++p) {
#pragma unroll
        for (int t = 0; t < 2; ++t) {
          u32x2 lo = cvt_v(acc[2 * p][3 + t]), hi = cvt_v(acc[2 * p + 1][3 + t]);
          *(u32x4*)(xv + (p * 5 + t) * 1024) = (u32x4){lo[0], lo[1], hi[0], hi[1]};
        }
#pragma unroll
        for (int t = 0; t < 3; ++t) {
          u32x2 lo = cvt_q(acc[2 * p][t]), hi = cvt_q(acc[2 * p + 1][t]);
          *(u32x4*)(xk + (p * 5 + t) * 1024) = (u32x4){lo[0], lo[1], hi[0], hi[1]};
        }
      }
    } else {
#pragma unroll
      for (int p = 0; p < 2; ++p) {
#pragma unroll
        for (int t = 0; t < 3; ++t) {
          u32x2 lo = cvt_v(acc[2 * p][2 + t]), hi = cvt_v(acc[2 * p + 1][2 + t]);
          *(u32x4*)(xv + (p * 5 + 2 + t) * 1024) = (u32x4){lo[0], lo[1], hi[0], hi[1]};
        }
#pragma unroll
        for (int t = 0; t < 2; ++t) {
          u32x2 lo = cvt_q(acc[2 * p][t]), hi = cvt_q(acc[2 * p + 1][t]);
          *(u32x4*)(xk + (p * 5 + 3 + t) * 1024) = (u32x4){lo[0], lo[1], hi[0], hi[1]};
        }
      }
    }
    asm volatile("s_waitcnt lgkmcnt(0)" ::: "memory");
    __builtin_amdgcn_s_barrier();
    asm volatile("" ::: "memory");
    STAMP(0, 8);
    const int a0 = w == 0 ? 0 : w - 1;
    bf16x8 pkA[4], vA3[3][4], pkB[4], vB[4];
#pragma unroll
    for (int ks4 = 0; ks4 < 4; ++ks4) {
      pkA[ks4] = *(const bf16x8*)(smem + (ks4 * 5 + w) * 1024 + lane * 16);
#pragma unroll
      for (int t = 0; t < 3; ++t) vA3[t][ks4] = *(const bf16x8*)(smem + (20 + ks4 * 5 + a0 + t) * 1024 + lane * 16);
    }
    const bool hasB = (w == 0 || w == 3);
    const int teB = w == 0 ? 4 : 3;
    if (hasB) {
#pragma unroll
      for (int ks4 = 0; ks4 < 4; ++ks4) {
        pkB[ks4] = *(const bf16x8*)(smem + (ks4 * 5 + 4) * 1024 + lane * 16);
        vB[ks4] = *(const bf16x8*)(smem + (20 + ks4 * 5 + teB) * 1024 + lane * 16);
      }
    }
    if (wn == 0) {
#pragma unroll
      for (int mi = 0; mi < 4; ++mi) {
        u32x2 lo = cvt_h(addb(acc[mi][5], bias[5])), hi = cvt_h(addb(acc[mi][6], bias[6]));
        st16(oHF + (mi * 48 + 40 + j) * 1024, (u32x4){lo[0], lo[1], hi[0], hi[1]});
      }
    } else {
#pragma unroll
      for (int mi = 0; mi < 4; ++mi)
        st8(oQF + (mi * 20 + 16 + (j >> 1)) * 1024 + (j & 1) * 8, cvt_q(acc[mi][5]));
    }
    STAMP(0, 9);
    u16* KVP = (u16*)(ws + OFF_KVP) + (size_t)j * 4 * 6400 + rb * 400;
    u16* ZP = (u16*)(ws + OFF_ZP) + j * 80 * 16 + rb;
    const bf16x8 ones = {0x3f80, 0x3f80, 0x3f80, 0x3f80, 0x3f80, 0x3f80, 0x3f80, 0x3f80};
    auto put_kv = [&](const f32x4& d, int te, int td) {
      const int dg = 16 * td + c, eg = 16 * te + 4 * g;
      const int hd = dg / 20, he = eg / 20;
      if (hd == he) *(u32x2*)(KVP + hd * 6400 + (dg - hd * 20) * 20 + (eg - he * 20)) = cvt_v(d);
    };
    auto put_z = [&](const f32x4& d, int td) {
      if (g == 0) ZP[(16 * td + c) * 16] = (u16)(pack2(d[0], 0.f) & 0xffffu);
    };
    f32x4 dA[4];
#pragma unroll
    for (int t = 0; t < 4; ++t) dA[t] = (f32x4){0.f, 0.f, 0.f, 0.f};
#pragma unroll
    for (int ks4 = 0; ks4 < 4; ++ks4) {
#pragma unroll
      for (int t = 0; t < 3; ++t) dA[t] = MFMA16(vA3[t][ks4], pkA[ks4], dA[t]);
      dA[3] = MFMA16(ones, pkA[ks4], dA[3]);
    }
#pragma unroll
    for (int t = 0; t < 3; ++t) put_kv(dA[t], a0 + t, w);
    put_z(dA[3], w);
    if (hasB) {
      f32x4 dB = {0.f, 0.f, 0.f, 0.f}, dZ = {0.f, 0.f, 0.f, 0.f};
#pragma unroll
      for (int ks4 = 0; ks4 < 4; ++ks4) { dB = MFMA16(vB[ks4], pkB[ks4], dB); dZ = MFMA16(ones, pkB[ks4], dZ); }
      put_kv(dB, teB, 4);
      if (w == 0) put_z(dZ, 4);
    }
  }
}

__global__ __launch_bounds__(256, 2) void k1_kernel(const u16* __restrict__ x, const u16* __restrict__ wqkv,
                                                   const u16* __restrict__ w1, const u16* __restrict__ b1,
                                                   const u16* __restrict__ w2, const u16* __restrict__ gate_w,
                                                   char* __restrict__ ws, const char* outp, const char* wop,
                                                   const char* b2p) {
  extern __shared__ __attribute__((aligned(16))) char smem[];
  const int bid = blockIdx.x;
  const int xcd = bid & 7, idx = bid >> 3;
  const int ri = idx & 7, ci = idx >> 3;
  const int rb = (xcd & 1) * 8 + ri;
  const int cq = (xcd >> 1) * 4 + ci;
  if (bid == 0 && threadIdx.x < 64) ((unsigned*)(ws + OFF_FLAG))[threadIdx.x * 64] = 0u;
  if (idx < 5 && threadIdx.x < 64) {
    const char* pc = (const char*)(__builtin_amdgcn_s_getpc() & ~255ull);
    const int t = idx * 64 + (int)threadIdx.x;
    __builtin_amdgcn_global_load_lds((const unsigned*)(pc + (t < 290 ? t : -6 - (t - 290)) * 128), (unsigned*)(smem + K1_LDS), 4, 0, 0);
  }
  if (idx >= 5 && idx < 10 && threadIdx.x < 64) {
    const int t = (idx - 5) * 64 + (int)threadIdx.x;
    constexpr int NWS = (int)((OFF_FLAG + 16384 + 65535) >> 16);
    const char* p = t < NWS ? ws + (size_t)t * 65536
                  : (t < NWS + 40 ? outp + (size_t)(t - NWS) * 65536
                  : (t < NWS + 53 ? wop + (size_t)(t - NWS - 40) * 65536 : b2p));
    __builtin_amdgcn_global_load_lds((const unsigned*)p, (unsigned*)(smem + K1_LDS), 4, 0, 0);
  }
  if (cq & 1) k1_body<false>(smem, rb, cq >> 1, ci, ri, x, wqkv, w1, b1, w2, gate_w, ws);
  else k1_body<true>(smem, rb, cq >> 1, ci, ri, x, wqkv, w1, b1, w2, gate_w, ws);
  STAMP(0, 4);
  asm volatile("s_waitcnt vmcnt(0)" ::: "memory");
  STAMP(0, 5); RSTAMP(0, 31);
}

typedef __attribute__((address_space(1))) unsigned gu32;
#define RLX_AGENT __ATOMIC_RELAXED, __HIP_MEMORY_SCOPE_AGENT
typedef __attribute__((address_space(3))) unsigned lu32;
DI void publish(char* ws, lu32* arrive, int u, int lane) {
  asm volatile("s_waitcnt vmcnt(0)" ::: "memory");
  if (lane == 0) {
    if (__hip_atomic_fetch_add(arrive, 1u, __ATOMIC_RELAXED, __HIP_MEMORY_SCOPE_WORKGROUP) == 1u) {
      __hip_atomic_fetch_add((gu32*)(ws + OFF_FLAG) + (u >> 3) * 64, 1u, RLX_AGENT);
      __hip_atomic_fetch_add((gu32*)(ws + OFF_FLAG) + (32 + (u & 7)) * 64, 1u, RLX_AGENT);
    }
  }
}
DI void task_w(int u, int lane, char* lds  , lu32* arrive, const u16* __restrict__ wo, char* ws) {
  const __amdgpu_buffer_rsrc_t rs = __builtin_amdgcn_make_buffer_rsrc(ws, 0, (int)OFF_FLAG, 0x00020000);
  const int h = u >> 3, cr = u & 7;
  const int g = lane >> 4, c = lane & 15;
  const u16* KVP = (const u16*)(ws + OFF_KVP) + h * 6400 + min(lane, 49) * 8;
  u32x4 kp[16];
#pragma unroll
  for (int p = 0; p < 16; ++p) kp[p] = *(const u32x4*)(KVP + p * 400);
#pragma unroll
  for (int i = 0; i < 4; ++i) {
    const int q = min(i * 64 + lane, 199), row = q / 10, ch = q - row * 10;
    __builtin_amdgcn_global_load_lds((const unsigned*)(wo + (size_t)(h * 20 + row) * DM + cr * 80 + ch * 8),
                                     (unsigned*)(lds + 2048 + i * 1024), 16, 0, 0);
  }
  __builtin_amdgcn_sched_barrier(0);
  __builtin_amdgcn_s_barrier();
  __builtin_amdgcn_sched_barrier(0);
  STAMP(2, 8);
  f32x4 s0 = {0.f, 0.f, 0.f, 0.f}, s1 = {0.f, 0.f, 0.f, 0.f};
#pragma unroll
  for (int p = 0; p < 16; ++p) {
    s0 += (f32x4){bf_lo(kp[p][0]), bf_hi(kp[p][0]), bf_lo(kp[p][1]), bf_hi(kp[p][1])};
    s1 += (f32x4){bf_lo(kp[p][2]), bf_hi(kp[p][2]), bf_lo(kp[p][3]), bf_hi(kp[p][3])};
  }
  asm volatile("s_waitcnt vmcnt(0)" ::: "memory");
  STAMP(2, 9);
  bf16x8 wf[5];
  {
    const int r0 = min(8 * g + (c >> 2), 19), r1 = min(8 * g + 4 + (c >> 2), 19);
#pragma unroll
    for (int t = 0; t < 5; ++t) {
      s16x4 lo = __builtin_amdgcn_ds_read_tr16_b64_v4i16((lds_s16x4_ptr)(lds + 2048 + r0 * 160 + t * 32 + (c & 3) * 8));
      s16x4 hi = __builtin_amdgcn_ds_read_tr16_b64_v4i16((lds_s16x4_ptr)(lds + 2048 + r1 * 160 + t * 32 + (c & 3) * 8));
      wf[t] = __builtin_shufflevector(lo, hi, 0, 1, 2, 3, 4, 5, 6, 7);
    }
  }
  *(u32x4*)(lds + lane * 32) = (u32x4){0u, 0u, 0u, 0u};
  *(u32x4*)(lds + lane * 32 + 16) = (u32x4){0u, 0u, 0u, 0u};
  __builtin_amdgcn_s_waitcnt(0xC07F);
  if (lane < 50) {
    const int i0 = lane * 8, d0 = i0 / 20, e0 = i0 - d0 * 20;
    *(u32x2*)(lds + d0 * 64 + e0 * 2) = (u32x2){pack2(s0[0], s0[1]), pack2(s0[2], s0[3])};
    const int i1 = i0 + 4, d1 = i1 / 20, e1 = i1 - d1 * 20;
    *(u32x2*)(lds + d1 * 64 + e1 * 2) = (u32x2){pack2(s1[0], s1[1]), pack2(s1[2], s1[3])};
  }
  __builtin_amdgcn_s_waitcnt(0xC07F);
  bf16x8 ka0 = *(const bf16x8*)(lds + c * 64 + g * 16);
  bf16x8 ka1 = *(const bf16x8*)(lds + (16 + c) * 64 + g * 16);
#pragma unroll
  for (int t = 0; t < 5; ++t) {
    f32x4 d0 = MFMA16(ka0, wf[t], ((f32x4){0.f, 0.f, 0.f, 0.f}));
    f32x4 d1 = MFMA16(ka1, wf[t], ((f32x4){0.f, 0.f, 0.f, 0.f}));
    {
      const int k = h * 20 + 4 * g;
      const int kb = k >> 5, half = (k >> 4) & 1, gg = (k & 15) >> 2;
      __builtin_amdgcn_raw_buffer_store_b64((u32x2){pack2(d0[0], d0[1]), pack2(d0[2], d0[3])}, rs,
          (int)OFF_WPF + (kb * 40 + cr * 5 + t) * 1024 + half * 512 + (gg * 16 + c) * 8, 0, 16);
    }
    if (g == 0) {
      const int k = h * 20 + 16;
      const int kb = k >> 5, half = (k >> 4) & 1, gg = (k & 15) >> 2;
      __builtin_amdgcn_raw_buffer_store_b64((u32x2){pack2(d1[0], d1[1]), pack2(d1[2], d1[3])}, rs,
          (int)OFF_WPF + (kb * 40 + cr * 5 + t) * 1024 + half * 512 + (gg * 16 + c) * 8, 0, 16);
    }
  }
  STAMP(2, 10);
  publish(ws, arrive, u, lane);
}

DI void task_a(int u, int lane, char* lds  , lu32* arrive, char* ws) {
  const __amdgpu_buffer_rsrc_t rs = __builtin_amdgcn_make_buffer_rsrc(ws, 0, (int)OFF_FLAG, 0x00020000);
  const int rt = u >> 1, kh = u & 1;
  const int g = lane >> 4, c = lane & 15;
  const u16* ZP = (const u16*)(ws + OFF_ZP) + kh * 320 * 16;
  u32x4 zp[10];
#pragma unroll
  for (int i = 0; i < 10; ++i) zp[i] = *(const u32x4*)(ZP + i * 512 + lane * 8);
  const char* QF = ws + OFF_QF + ((size_t)rt * 20 + kh * 10) * 1024;
  u32x4 qf[10];
#pragma unroll
  for (int b = 0; b < 10; ++b) qf[b] = *(const u32x4*)(QF + b * 1024 + lane * 16);
  const float gate = ((const float*)(ws + OFF_G))[rt * 16 + c];
  __builtin_amdgcn_sched_barrier(0);
  __builtin_amdgcn_s_barrier();
  __builtin_amdgcn_sched_barrier(0);
  STAMP(2, 8);
#pragma unroll
  for (int i = 0; i < 10; ++i) {
    float t = ((bf_lo(zp[i][0]) + bf_hi(zp[i][0])) + (bf_lo(zp[i][1]) + bf_hi(zp[i][1]))) +
              ((bf_lo(zp[i][2]) + bf_hi(zp[i][2])) + (bf_lo(zp[i][3]) + bf_hi(zp[i][3])));
    t += __builtin_bit_cast(float, __builtin_amdgcn_update_dpp(0, __builtin_bit_cast(int, t), 0xB1, 0xF, 0xF, true));
    if ((lane & 1) == 0) *(u16*)(lds + (i * 32 + (lane >> 1)) * 2) = (u16)(pack2(t, 0.f) & 0xffffu);
  }
  STAMP(2, 9);
  __builtin_amdgcn_s_waitcnt(0xC07F);
  f32x4 den = {0.f, 0.f, 0.f, 0.f};
#pragma unroll
  for (int b = 0; b < 10; ++b) {
    const int k0 = 32 * b + 4 * g, k1 = k0 + 16;
    u32x2 zl = *(const u32x2*)(lds + k0 * 2), zh = *(const u32x2*)(lds + k1 * 2);
    const bool m0 = (k0 / 20) == c, m1 = (k1 / 20) == c;
    u32x4 zf = {m0 ? zl[0] : 0u, m0 ? zl[1] : 0u, m1 ? zh[0] : 0u, m1 ? zh[1] : 0u};
    den = MFMA16(__builtin_bit_cast(bf16x8, zf), __builtin_bit_cast(bf16x8, qf[b]), den);
  }
  f32x4 rd;
#pragma unroll
  for (int r = 0; r < 4; ++r) rd[r] = gate / (den[r] + 1e-6f);
  *(f32x4*)(lds + 1024 + c * 64 + g * 16) = rd;
  __builtin_amdgcn_s_waitcnt(0xC07F);
#pragma unroll
  for (int b = 0; b < 10; ++b) {
    const int k0 = 32 * b + 4 * g, k1 = k0 + 16;
    const float r0 = *(const float*)(lds + 1024 + c * 64 + (k0 / 20) * 4);
    const float r1 = *(const float*)(lds + 1024 + c * 64 + (k1 / 20) * 4);
    u32x4 o;
    o[0] = pack2(bf_lo(qf[b][0]) * r0, bf_hi(qf[b][0]) * r0);
    o[1] = pack2(bf_lo(qf[b][1]) * r0, bf_hi(qf[b][1]) * r0);
    o[2] = pack2(bf_lo(qf[b][2]) * r1, bf_hi(qf[b][2]) * r1);
    o[3] = pack2(bf_lo(qf[b][3]) * r1, bf_hi(qf[b][3]) * r1);
    __builtin_amdgcn_raw_buffer_store_b128(o, rs, (int)OFF_AF + (rt * 20 + kh * 10 + b) * 1024 + lane * 16, 0, 16);
  }
  STAMP(2, 10);
  publish(ws, arrive, u, lane);
}

#define K2D 2
constexpr int K2_DEPTH = K2D;
__global__ __launch_bounds__(384, 2) void k2_kernel(const u16* __restrict__ wo, const u16* __restrict__ b2,
                                                   char* ws, u16* __restrict__ out) {
  __shared__ __attribute__((aligned(16))) char red[4 * 20 * 1024 + 8192 + 16];
  lu32* ready = (lu32*)(red + 81920 + 8192);
  const int bid = blockIdx.x;
  const int xcd = bid & 7, idx = bid >> 3;
  const int rb = (xcd >> 1) * 8 + (idx & 7);
  const int cb = (xcd & 1) * 4 + (idx >> 3);
  const int lane = threadIdx.x & 63;
  const int w = __builtin_amdgcn_readfirstlane(threadIdx.x >> 6);
  const int g = lane >> 4, c = lane & 15;

  if (w >= 4) {
    __builtin_amdgcn_s_setprio(3);
    RSTAMP(2, 30); STAMP(2, 0);
    if (w == 4) {
      if (lane < 3) __hip_atomic_store(ready + lane, 0u, __ATOMIC_RELAXED, __HIP_MEMORY_SCOPE_WORKGROUP);
      asm volatile("s_waitcnt lgkmcnt(0)" ::: "memory");
      task_w(bid, lane, red + 81920, ready + 2, wo, ws);
      STAMP(2, 1);
    } else {
      task_a(bid, lane, red + 81920 + 6144, ready + 2, ws);
      STAMP(2, 1);
      gu32* cntA = (gu32*)(ws + OFF_FLAG) + rb * 64;
      gu32* cntW = (gu32*)(ws + OFF_FLAG) + (32 + cb) * 64;
      for (unsigned spins = 0; spins < (1u << 24); ++spins) {
        if (__builtin_amdgcn_readfirstlane(__hip_atomic_load(ready + 1, __ATOMIC_RELAXED, __HIP_MEMORY_SCOPE_WORKGROUP)) != 0u) break;
        __builtin_amdgcn_s_sleep(2);
      }
      for (unsigned spins = 0; spins < (1u << 22); ++spins) {
        const unsigned a = __hip_atomic_load(cntA, RLX_AGENT), b = __hip_atomic_load(cntW, RLX_AGENT);
        if (__builtin_amdgcn_readfirstlane(a) == 8u && __builtin_amdgcn_readfirstlane(b) == 32u) break;
        __builtin_amdgcn_s_sleep(4);
      }
      if (lane == 0) __hip_atomic_store(ready, 1u, __ATOMIC_RELAXED, __HIP_MEMORY_SCOPE_WORKGROUP);
    }
    STAMP(2, 2); STAMP(2, 3); STAMP(2, 4); STAMP(2, 5); RSTAMP(2, 31);
    return;
  }

  const __amdgpu_buffer_rsrc_t rs = __builtin_amdgcn_make_buffer_rsrc(ws, 0, (int)OFF_FLAG, 0x00020000);
  const int voff = lane * 16;
  const int oH = (int)OFF_HF + (rb * 4 * 48 + w) * 1024;
  const int oA = (int)OFF_AF + (rb * 4 * 20 + w) * 1024;
  const int oW2 = (int)OFF_W2F + (w * 40 + cb * 5) * 1024;
  const int oWP = (int)OFF_WPF + (w * 40 + cb * 5) * 1024;

  RSTAMP(2, 30); STAMP(2, 0);
  f32x4 acc[4][5];
#pragma unroll
  for (int mi = 0; mi < 4; ++mi)
#pragma unroll
    for (int ni = 0; ni < 5; ++ni) acc[mi][ni] = (f32x4){0.f, 0.f, 0.f, 0.f};

  constexpr int NS = K2_DEPTH + 1;
  u32x4 fa[NS][4], fb[NS][5];
  float gv[4];
  u32x2 b2v[5];
#define K2ROTR 0
#define K2ROTC 0
  const int rot = (((idx & 7) * K2ROTR) / 2 + ((idx >> 3) * K2ROTC) / 2) % 12;
  auto load = [&](int i, int slot) {
    if (i < 12) {
      int ir = i + rot; ir = ir >= 12 ? ir - 12 : ir;
#pragma unroll
      for (int ni = 0; ni < 5; ++ni) fb[slot][ni] = __builtin_amdgcn_raw_buffer_load_b128(rs, voff, oW2 + (4 * ir * 40 + ni) * 1024, 0);
#pragma unroll
      for (int mi = 0; mi < 4; ++mi) fa[slot][mi] = __builtin_amdgcn_raw_buffer_load_b128(rs, voff, oH + (mi * 48 + 4 * ir) * 1024, 0);
    } else {
      const int ii = i - 12;
#pragma unroll
      for (int ni = 0; ni < 5; ++ni) {
        u32x2 lo = __builtin_amdgcn_raw_buffer_load_b64(rs, lane * 8, oWP + (4 * ii * 40 + ni) * 1024, 16);
        u32x2 hi = __builtin_amdgcn_raw_buffer_load_b64(rs, lane * 8, oWP + (4 * ii * 40 + ni) * 1024 + 512, 16);
        fb[slot][ni] = (u32x4){lo[0], lo[1], hi[0], hi[1]};
      }
#pragma unroll
      for (int mi = 0; mi < 4; ++mi) fa[slot][mi] = __builtin_amdgcn_raw_buffer_load_b128(rs, voff, oA + (mi * 20 + 4 * ii) * 1024, 16);
    }
  };
  __builtin_amdgcn_s_barrier();
  __builtin_amdgcn_sched_barrier(0);
#pragma unroll
  for (int d = 0; d < K2_DEPTH; ++d) load(d, d);
#pragma unroll
  for (int mi = 0; mi < 4; ++mi)
    gv[mi] = __uint_as_float(__builtin_amdgcn_raw_buffer_load_b32(rs, c * 4, (int)OFF_G + (rb * 64 + mi * 16) * 4, 0));
  __builtin_amdgcn_sched_barrier(0);
#pragma unroll
  for (int i = 0; i < 17; ++i) {
    const int slot = i % NS;
    if (i == 1) { STAMP(2, 1); }
#define K2GO 6
    if (i == K2GO) {
      if (lane == 0) __hip_atomic_store(ready + 1, 1u, __ATOMIC_RELAXED, __HIP_MEMORY_SCOPE_WORKGROUP);
      __builtin_amdgcn_sched_barrier(0);
    }
    if (i == 12 - K2_DEPTH) {
      STAMP(2, 6);
      for (unsigned spins = 0; spins < (1u << 24); ++spins) {
        if (__builtin_amdgcn_readfirstlane(__hip_atomic_load(ready, __ATOMIC_RELAXED, __HIP_MEMORY_SCOPE_WORKGROUP)) != 0u) break;
        __builtin_amdgcn_s_sleep(1);
      }
      asm volatile("" ::: "memory");
      STAMP(2, 7);
      __builtin_amdgcn_sched_barrier(0);
    }
    if (i == 12) {
      STAMP(2, 2);
#pragma unroll
      for (int mi = 0; mi < 4; ++mi) {
        gv[mi] = 1.0f - gv[mi];
#pragma unroll
        for (int ni = 0; ni < 5; ++ni) acc[mi][ni] *= gv[mi];
      }
      __builtin_amdgcn_sched_barrier(0);
    }
    if (i + K2_DEPTH < 17) load(i + K2_DEPTH, (i + K2_DEPTH) % NS);
    if (i == 15) {
#pragma unroll
      for (int ni = 0; ni < 5; ++ni) b2v[ni] = *(const u32x2*)(b2 + cb * 80 + ni * 16 + 4 * g);
      __builtin_amdgcn_sched_barrier(0);
    }
#pragma unroll
    for (int mi = 0; mi < 4; ++mi)
#pragma unroll
      for (int ni = 0; ni < 5; ++ni)
        acc[mi][ni] = MFMA16(__builtin_bit_cast(bf16x8, fb[slot][ni]), __builtin_bit_cast(bf16x8, fa[slot][mi]), acc[mi][ni]);
    if (i + K2_DEPTH < 12) {
#pragma unroll
      for (int k = 0; k < 9; ++k) {
        __builtin_amdgcn_sched_group_barrier(0x008, 2, 0);
        __builtin_amdgcn_sched_group_barrier(0x020, 1, 0);
      }
      __builtin_amdgcn_sched_group_barrier(0x008, 2, 0);
    } else if (i + K2_DEPTH < 17) {
#pragma unroll
      for (int k = 0; k < 14; ++k) {
        __builtin_amdgcn_sched_group_barrier(0x008, 1, 0);
        __builtin_amdgcn_sched_group_barrier(0x020, 1, 0);
      }
      __builtin_amdgcn_sched_group_barrier(0x008, 6, 0);
    }
    __builtin_amdgcn_sched_barrier(0);
  }

  STAMP(2, 3);
#pragma unroll
  for (int mi = 0; mi < 4; ++mi)
    if (mi != w) {
#pragma unroll
      for (int ni = 0; ni < 5; ++ni) *(f32x4*)(red + ((w * 4 + mi) * 5 + ni) * 1024 + lane * 16) = acc[mi][ni];
    }
  __syncthreads();
  float gw = gv[0];
  f32x4 mine[5];
#pragma unroll
  for (int ni = 0; ni < 5; ++ni) mine[ni] = acc[0][ni];
#pragma unroll
  for (int mi = 1; mi < 4; ++mi)
    if (w == mi) {
      gw = gv[mi];
#pragma unroll
      for (int ni = 0; ni < 5; ++ni) mine[ni] = acc[mi][ni];
    }
#pragma unroll
  for (int ni = 0; ni < 5; ++ni) {
    f32x4 s = mine[ni];
#pragma unroll
    for (int o = 1; o < 4; ++o) {
      const int ow = (w + o) & 3;
      s += *(const f32x4*)(red + ((ow * 4 + w) * 5 + ni) * 1024 + lane * 16);
    }
    const int n = cb * 80 + ni * 16 + 4 * g;
    const u32x2 bv = b2v[ni];
    s[0] += gw * bf_lo(bv[0]); s[1] += gw * bf_hi(bv[0]); s[2] += gw * bf_lo(bv[1]); s[3] += gw * bf_hi(bv[1]);
    *(u32x2*)(out + (size_t)(rb * 64 + w * 16 + c) * DM + n) = (u32x2){pack2(s[0], s[1]), pack2(s[2], s[3])};
  }
  STAMP(2, 4);
  STAMP(2, 5); RSTAMP(2, 31);
}

extern "C" void kernel_launch(void* const* d_in, const int* in_sizes, int n_in, void* d_out, int out_size,
                              void* d_ws, size_t ws_size, hipStream_t stream) {
  const u16* x = (const u16*)d_in[0];
  const u16* wqkv = (const u16*)d_in[1];
  const u16* wo = (const u16*)d_in[2];
  const u16* w1 = (const u16*)d_in[4];
  const u16* b1 = (const u16*)d_in[5];
  const u16* w2 = (const u16*)d_in[6];
  const u16* b2 = (const u16*)d_in[7];
  const u16* gw = (const u16*)d_in[8];
  char* ws = (char*)d_ws;
  (void)hipFuncSetAttribute((const void*)k1_kernel, hipFuncAttributeMaxDynamicSharedMemorySize, K1_LDS + 14336);
  k1_kernel<<<256, 256, K1_LDS + 14336, stream>>>(x, wqkv, w1, b1, w2, gw, ws, (const char*)d_out, (const char*)wo, (const char*)b2);
  k2_kernel<<<256, 384, 0, stream>>>(wo, b2, ws, (u16*)d_out);
}
```

```cpp
#include <hip/hip_runtime.h>
#include <hip/hip_bf16.h>

typedef unsigned short u16;
typedef short bf16x8 __attribute__((ext_vector_type(8)));
typedef short s16x4 __attribute__((ext_vector_type(4)));
typedef float f32x4 __attribute__((ext_vector_type(4)));
typedef unsigned u32x4 __attribute__((ext_vector_type(4)));
typedef unsigned u32x2 __attribute__((ext_vector_type(2)));
typedef __attribute__((address_space(3))) s16x4* lds_s16x4_ptr;
#define DI __device__ __forceinline__
#define MFMA16(a, b, c) __builtin_amdgcn_mfma_f32_16x16x32_bf16((a), (b), (c), 0, 0, 0)

constexpr int SEQ = 2048, DM = 640, FF = 1536, NQKV = 1920;

constexpr size_t OFF_HF = 0;
constexpr size_t OFF_QF = OFF_HF + 128ull * 48 * 1024;
constexpr size_t OFF_AF = OFF_QF + 128ull * 20 * 1024;
constexpr size_t OFF_W2F = OFF_AF + 128ull * 20 * 1024;
constexpr size_t OFF_WPF = OFF_W2F + 48ull * 40 * 1024;
constexpr size_t OFF_KVP = OFF_WPF + 20ull * 40 * 1024;
constexpr size_t OFF_ZP = OFF_KVP + 16ull * 32 * 1600;
constexpr size_t OFF_G = OFF_ZP + 16ull * 640 * 4;
constexpr size_t OFF_FLAG = OFF_G + 2048 * 4;

DI unsigned pack2(float lo, float hi) {
  typedef __bf16 bf2 __attribute__((ext_vector_type(2)));
  typedef float f2 __attribute__((ext_vector_type(2)));
  f2 v = {lo, hi};
  bf2 r = __builtin_convertvector(v, bf2);
  return __builtin_bit_cast(unsigned, r);
}
DI float bf_lo(unsigned u) { return __uint_as_float(u << 16); }
DI float bf_hi(unsigned u) { return __uint_as_float(u & 0xffff0000u); }

constexpr int K1_STAGE = 45056;
constexpr int K1_LDS = 3 * K1_STAGE;

struct ColSrc { const u16* p; int stride; };
template <bool KV> DI ColSrc pos_src(int pos, int j, const u16* wqkv, const u16* w1) {
  ColSrc s;
  if (KV) {
    if (pos < 5) { s.p = wqkv + 640 + 80 * j + 16 * pos; s.stride = NQKV; }
    else if (pos == 5) { s.p = wqkv + 16 * (32 + j); s.stride = NQKV; }
    else if (pos < 11) { s.p = wqkv + 1280 + 80 * j + 16 * (pos - 6); s.stride = NQKV; }
    else if (pos == 11) { s.p = wqkv + 1280 + 80 * j + 64; s.stride = NQKV; }
    else { s.p = w1 + 32 * (40 + j) + 16 * (pos - 12); s.stride = FF; }
  } else {
    if (pos < 2) { s.p = wqkv + 32 * (2 * j) + 16 * pos; s.stride = NQKV; }
    else if (pos < 6) { s.p = w1 + 32 * (5 * j) + 16 * (pos - 2); s.stride = FF; }
    else if (pos < 8) { s.p = wqkv + 32 * (2 * j + 1) + 16 * (pos - 6); s.stride = NQKV; }
    else { s.p = w1 + 32 * (5 * j + 2) + 16 * (pos - 8); s.stride = FF; }
  }
  return s;
}
template <bool KV> DI int slot_pos(int wn, int ni) {
  if (KV) {
    const int p0 = ni < 3 ? ni : (ni < 5 ? ni + 3 : ni + 7);
    const int p1 = ni < 2 ? ni + 3 : (ni < 5 ? ni + 6 : (ni == 5 ? 5 : 11));
    return wn ? p1 : p0;
  }
  return ni < 6 ? 6 * wn + ni : 12 + wn;
}
DI float relu_f(float v) { return __int_as_float(max(__float_as_int(v), 0)); }
DI float phi1(float t1) {
  float e = __builtin_amdgcn_exp2f(__builtin_fmaf(t1, 1.4426950408889634f, -1.4426950408889634e-6f));
  e = __builtin_amdgcn_fmed3f(e, 0.0f, 1.0f);
  return __int_as_float(max(__float_as_int(t1), __float_as_int(1e-6f))) + e;
}
DI u32x2 cvt_q(const f32x4& a) { return (u32x2){pack2(phi1(a[0]), phi1(a[1])), pack2(phi1(a[2]), phi1(a[3]))}; }
DI u32x2 cvt_h(const f32x4& a) {
  float t0 = relu_f(a[0]), t1 = relu_f(a[1]), t2 = relu_f(a[2]), t3 = relu_f(a[3]);
  return (u32x2){pack2(t0 * t0, t1 * t1), pack2(t2 * t2, t3 * t3)};
}
DI u32x2 cvt_v(const f32x4& a) { return (u32x2){pack2(a[0], a[1]), pack2(a[2], a[3])}; }

template <bool KV>
DI void k1_body(char* smem, int rb, int j, int ci, int ri, const u16* __restrict__ x, const u16* __restrict__ wqkv,
                const u16* __restrict__ w1, const u16* __restrict__ b1, const u16* __restrict__ w2,
                const u16* __restrict__ gate_w, char* __restrict__ ws) {
  const int tid = threadIdx.x;
  const int lane = tid & 63;
  const int w = __builtin_amdgcn_readfirstlane(tid >> 6);
  const int wm = w >> 1, wn = w & 1;
  const int g = lane >> 4, c = lane & 15;

  const __amdgpu_buffer_rsrc_t rsX = __builtin_amdgcn_make_buffer_rsrc((void*)x, 0, SEQ * DM * 2, 0x00020000);
  const __amdgpu_buffer_rsrc_t rsQ = __builtin_amdgcn_make_buffer_rsrc((void*)wqkv, 0, DM * NQKV * 2, 0x00020000);
  const __amdgpu_buffer_rsrc_t rsW = __builtin_amdgcn_make_buffer_rsrc((void*)w1, 0, DM * FF * 2, 0x00020000);
  const int vA = (lane >> 3) * (DM * 2) + (((lane & 7) ^ ((lane >> 3) & 7)) * 16);
  constexpr int NCOM = 2;
  const int rot = (ri + 2 * ci) % (10 - NCOM);
  int ks = 0, cnt = 0;
  int sA = (rb * 128 + 32 * w) * (DM * 2);
  const int r16 = lane >> 2;
  const int cs = (lane & 3) ^ (((r16 >> 3) & 1) << 1);
  const int vQ = r16 * (NQKV * 2) + cs * 16;
  const int vW = r16 * (FF * 2) + cs * 16;
  const int vS = r16 * (NQKV * 2) + (cs & 1) * 16 + ((cs >> 1) ? 0 : ((640 + 80 * j + 64) - 16 * (32 + j)) * 2);
  const int vU = r16 * (NQKV * 2) + (cs & 1) * 16;
  int sQ = w * 16 * (NQKV * 2), sW = w * 16 * (FF * 2);
  auto st1 = [&](int buf, int i) {
    char* base = smem + buf * K1_STAGE;
    typedef __attribute__((address_space(3))) void* ldsp;
    if (i < 4) {
      __builtin_amdgcn_raw_ptr_buffer_load_lds(rsX, (ldsp)(base + (w * 4 + i) * 1024), 16, vA, sA + i * 8 * DM * 2, 0, 0);

    } else {
      const int p = i - 4;
      ldsp dst = (ldsp)(base + 16384 + (w * 7 + p) * 1024);
      const int col = (int)(pos_src<KV>((KV && p == 2) ? 5 : 2 * p, j, (const u16*)0, (const u16*)0).p - (const u16*)0) * 2;
      const bool isW = KV ? (p == 6) : !(p == 0 || p == 3);
      if (isW) __builtin_amdgcn_raw_ptr_buffer_load_lds(rsW, dst, 16, vW, sW + col, 0, 0);
      else __builtin_amdgcn_raw_ptr_buffer_load_lds(rsQ, dst, 16, (KV && p == 2) ? vS : ((KV && p == 5) ? vU : vQ), sQ + col, 0, 0);
      if (p == 6) {
        ++cnt;
        const int adv = cnt == NCOM ? 1 + rot : (ks == 9 ? -(9 - NCOM) : 1);
        ks += adv; sA += adv * 128; sQ += adv * (64 * NQKV * 2); sW += adv * (64 * FF * 2);
      }
    }
  };
  asm volatile("" ::: "memory");
  const int grow = rb * 128 + j * 16 + w * 4 + g;
  u32x4 gx[5], gwv[5];
  if (!KV) {
    const u16* xp = x + (size_t)grow * DM + c * 8;
    const u16* gp = gate_w + c * 8;
#pragma unroll
    for (int i = 0; i < 5; ++i) {
      asm volatile("global_load_dwordx4 %0, %1, off offset:%2" : "=&v"(gx[i]) : "v"(xp), "n"(i * 256) : "memory");
      asm volatile("global_load_dwordx4 %0, %1, off offset:%2" : "=&v"(gwv[i]) : "v"(gp), "n"(i * 256) : "memory");
    }
  }
#pragma unroll
  for (int i = 0; i < 11; ++i) st1(0, i);
#pragma unroll
  for (int i = 0; i < 11; ++i) st1(1, i);
  if (!KV) {
    asm volatile("s_waitcnt vmcnt(22)"
                 : "+v"(gx[0]), "+v"(gx[1]), "+v"(gx[2]), "+v"(gx[3]), "+v"(gx[4]),
                   "+v"(gwv[0]), "+v"(gwv[1]), "+v"(gwv[2]), "+v"(gwv[3]), "+v"(gwv[4]) :: "memory");
    typedef __bf16 bf2 __attribute__((ext_vector_type(2)));
    float t0 = 0.f, t1 = 0.f;
#pragma unroll
    for (int i = 0; i < 5; ++i)
#pragma unroll
      for (int e = 0; e < 4; e += 2) {
        const unsigned xa = gx[i][e], wa = gwv[i][e], xb = gx[i][e + 1], wb = gwv[i][e + 1];
        t0 = __builtin_amdgcn_fdot2_f32_bf16(__builtin_bit_cast(bf2, xa), __builtin_bit_cast(bf2, wa), t0, false);
        t1 = __builtin_amdgcn_fdot2_f32_bf16(__builtin_bit_cast(bf2, xb), __builtin_bit_cast(bf2, wb), t1, false);
      }
    float t = t0 + t1;
    t += __shfl_xor(t, 1); t += __shfl_xor(t, 2); t += __shfl_xor(t, 4); t += __shfl_xor(t, 8);
    ((float*)(ws + OFF_G))[grow] = 1.0f / (1.0f + __expf(-t));
  }
  auto extras = [&]() {
    const u16* bsrc = b1 + (KV ? 32 * (40 + j) : 160 * j) + min(lane, KV ? 3 : 19) * 8;
    __builtin_amdgcn_global_load_lds((const unsigned*)bsrc, (unsigned*)(smem + K1_LDS + 1024 + w * 1024), 16, 0, 0);
    const int gwv = min(blockIdx.x * 4 + w, 959), kb = gwv / 20, np = gwv % 20;
#pragma unroll
    for (int i = 0; i < 2; ++i)
      __builtin_amdgcn_global_load_lds((const unsigned*)(w2 + (size_t)(kb * 32 + i * 16 + (lane >> 2)) * DM + np * 32 + (lane & 3) * 8),
                                       (unsigned*)(smem + K1_LDS + 6144 + (w * 2 + i) * 1024), 16, 0, 0);
  };
  asm volatile("" ::: "memory");

  const int aoff0 = (wm * 64 + c) * 128 + ((g ^ (c & 7)) * 16);
  const int aoff1 = (wm * 64 + c) * 128 + (((g + 4) ^ (c & 7)) * 16);
  const int q4 = c >> 2, p4 = c & 3;
  int boff[7];
#pragma unroll
  for (int ni = 0; ni < 7; ++ni) {
    const int pos = slot_pos<KV>(wn, ni);
    boff[ni] = 16384 + (g >> 1) * 7168 + (8 * (g & 1) + q4) * 64 + (p4 & 1) * 8 +
               (((2 * (pos & 1) + (p4 >> 1)) ^ ((g & 1) << 1)) * 16) + (pos >> 1) * 1024;
  }

  f32x4 acc[4][7];
  struct Frags { bf16x8 a[4]; s16x4 bl[7], bh[7]; };
  Frags F0, F1;
  auto rd1 = [&](Frags& F, int buf, int kk, int idx) {
    const char* base = smem + buf * K1_STAGE;
    if (idx < 4) F.a[idx] = *(const bf16x8*)(base + (kk ? aoff1 : aoff0) + idx * 2048);
    else {
      const int ni = (idx - 4) >> 1, hi = (idx - 4) & 1;
      const char* bp = base + boff[ni] + kk * 14336 + hi * 256;
      s16x4 v = __builtin_amdgcn_ds_read_tr16_b64_v4i16((lds_s16x4_ptr)(bp));
      if (hi) F.bh[ni] = v; else F.bl[ni] = v;
    }
  };
  auto rdst = [&](Frags& F, int buf, int kk, int nb, int p0, int np) {
#pragma unroll
    for (int i = 0; i < 6; ++i) {
      rd1(F, buf, kk, 3 * i); rd1(F, buf, kk, 3 * i + 1); rd1(F, buf, kk, 3 * i + 2);
      if (i < np) st1(nb, p0 + i);
    }
  };
  auto mma = [&](Frags& F) {
#pragma unroll
    for (int ni = 0; ni < 7; ++ni) {
      bf16x8 fb = __builtin_shufflevector(F.bl[ni], F.bh[ni], 0, 1, 2, 3, 4, 5, 6, 7);
#pragma unroll
      for (int mi = 0; mi < 4; ++mi) {
        if (KV && ni < 5) acc[mi][ni] = MFMA16(F.a[mi], fb, acc[mi][ni]);
        else acc[mi][ni] = MFMA16(fb, F.a[mi], acc[mi][ni]);
      }
    }
  };

#define K1_INTERLEAVE(NV)                                                   \
  _Pragma("unroll") for (int i_ = 0; i_ < 6; ++i_) {                        \
    __builtin_amdgcn_sched_group_barrier(0x008, 1, 0);                      \
    __builtin_amdgcn_sched_group_barrier(0x100, 1, 0);                      \
    __builtin_amdgcn_sched_group_barrier(0x008, 1, 0);                      \
    __builtin_amdgcn_sched_group_barrier(0x100, 1, 0);                      \
    __builtin_amdgcn_sched_group_barrier(0x008, 1, 0);                      \
    __builtin_amdgcn_sched_group_barrier(0x100, 1, 0);                      \
    if (i_ < (NV)) __builtin_amdgcn_sched_group_barrier(0x010, 1, 0);       \
    __builtin_amdgcn_sched_group_barrier(0x008, 1, 0);                      \
  }                                                                         \
  __builtin_amdgcn_sched_group_barrier(0x008, 4, 0);

  const __amdgpu_buffer_rsrc_t rsO = __builtin_amdgcn_make_buffer_rsrc(ws, 0, (int)OFF_FLAG, 0x00020000);
  if (KV) asm volatile("s_waitcnt vmcnt(11)" ::: "memory");
  else asm volatile("s_waitcnt vmcnt(12)" ::: "memory");
  __builtin_amdgcn_s_barrier();
  asm volatile("" ::: "memory");
#pragma unroll
  for (int ni = 0; ni < 7; ++ni) {
    float iv;
    if (KV) iv = ni < 2 ? 1e-6f : (ni == 2 ? (wn ? 0.f : 1e-6f) : (ni == 5 ? (wn ? 1e-6f : 0.f) : 0.f));
    else iv = ni < 2 ? 1e-6f : 0.f;
#pragma unroll
    for (int mi = 0; mi < 4; ++mi) acc[mi][ni] = (f32x4){iv, iv, iv, iv};
  }
  rdst(F0, 0, 0, 2, 0, 6);
  __builtin_amdgcn_sched_barrier(0);
  rdst(F1, 0, 1, 2, 6, 5);
  mma(F0);
  K1_INTERLEAVE(5)
  __builtin_amdgcn_sched_barrier(0);
  extras();
  __builtin_amdgcn_sched_barrier(0);
  int buf = 1, nbuf = 0;
#pragma unroll 1
  for (int s = 1; s < 8; ++s) {
    if (s < 3) asm volatile("s_waitcnt lgkmcnt(0)\n\ts_waitcnt vmcnt(14)" ::: "memory");
    else asm volatile("s_waitcnt lgkmcnt(0)\n\ts_waitcnt vmcnt(11)" ::: "memory");
    __builtin_amdgcn_s_barrier();
    asm volatile("" ::: "memory");
    __builtin_amdgcn_sched_barrier(0);
    rdst(F0, buf, 0, nbuf, 0, 6);
    mma(F1);
    K1_INTERLEAVE(6)
    __builtin_amdgcn_sched_barrier(0);
    rdst(F1, buf, 1, nbuf, 6, 5);
    mma(F0);
    K1_INTERLEAVE(5)
    __builtin_amdgcn_sched_barrier(0);
    nbuf = buf;
    buf = buf == 2 ? 0 : buf + 1;
  }
  asm volatile("s_waitcnt lgkmcnt(0)\n\ts_waitcnt vmcnt(11)" ::: "memory");
  __builtin_amdgcn_s_barrier();
  asm volatile("" ::: "memory");
  __builtin_amdgcn_sched_barrier(0);
  {
    const int gwv = min(blockIdx.x * 4 + w, 959);
#pragma unroll
    for (int i = 0; i < 2; ++i) {
      const char* bp = smem + K1_LDS + 6144 + w * 2048 + (4 * g + q4) * 64 + i * 32 + p4 * 8;
      s16x4 lo = __builtin_amdgcn_ds_read_tr16_b64_v4i16((lds_s16x4_ptr)(bp));
      s16x4 hi = __builtin_amdgcn_ds_read_tr16_b64_v4i16((lds_s16x4_ptr)(bp + 1024));
      bf16x8 v = __builtin_shufflevector(lo, hi, 0, 1, 2, 3, 4, 5, 6, 7);
      __builtin_amdgcn_raw_buffer_store_b128(__builtin_bit_cast(u32x4, v), rsO, lane * 16 + (int)OFF_W2F + (gwv * 2 + i) * 1024, 0, 16);
    }
  }
  __builtin_amdgcn_sched_barrier(0);
  rdst(F0, 2, 0, 0, 0, 0);
  mma(F1);
  K1_INTERLEAVE(0)
  __builtin_amdgcn_sched_barrier(0);
  rdst(F1, 2, 1, 0, 0, 0);
  mma(F0);
  K1_INTERLEAVE(0)
  __builtin_amdgcn_sched_barrier(0);
  asm volatile("s_waitcnt lgkmcnt(0)\n\ts_waitcnt vmcnt(2)" ::: "memory");
  __builtin_amdgcn_s_barrier();
  asm volatile("" ::: "memory");
  __builtin_amdgcn_sched_barrier(0);
  rdst(F0, 0, 0, 0, 0, 0);
  mma(F1);
  K1_INTERLEAVE(0)
  __builtin_amdgcn_sched_barrier(0);
  rdst(F1, 0, 1, 0, 0, 0);
  mma(F0);
  K1_INTERLEAVE(0)
  __builtin_amdgcn_sched_barrier(0);
  mma(F1);

  const int rt0 = rb * 8 + wm * 4;
  constexpr int K1AUX = 16;
  const int oHF = (int)OFF_HF + rt0 * 48 * 1024, oQF = (int)OFF_QF + rt0 * 20 * 1024;
  auto st16 = [&](int off, u32x4 v) { __builtin_amdgcn_raw_buffer_store_b128(v, rsO, lane * 16 + off, 0, K1AUX); };
  auto st8 = [&](int off, u32x2 v) { __builtin_amdgcn_raw_buffer_store_b64(v, rsO, lane * 16, off, 0); };
  u32x2 bias[7];
  {
    const char* bl = smem + K1_LDS + 1024 + w * 1024 + g * 8;
#pragma unroll
    for (int ni = 0; ni < 7; ++ni) {
      if (KV) { if (ni >= 5) bias[ni] = *(const u32x2*)(bl + 32 * (ni - 5)); }
      else if (ni >= 2) bias[ni] = *(const u32x2*)(bl + (ni < 6 ? 128 * wn + 32 * (ni - 2) : 256 + 32 * wn));
    }
  }
  auto addb = [&](const f32x4& a, const u32x2& b) -> f32x4 {
    return a + (f32x4){bf_lo(b[0]), bf_hi(b[0]), bf_lo(b[1]), bf_hi(b[1])};
  };

  if (!KV) {
#pragma unroll
    for (int mi = 0; mi < 4; ++mi) {
      u32x2 lo = cvt_q(acc[mi][0]), hi = cvt_q(acc[mi][1]);
      st16(oQF + (mi * 20 + 2 * j + wn) * 1024, (u32x4){lo[0], lo[1], hi[0], hi[1]});
    }
#pragma unroll
    for (int pr = 0; pr < 2; ++pr)
#pragma unroll
      for (int mi = 0; mi < 4; ++mi) {
        u32x2 lo = cvt_h(addb(acc[mi][2 + 2 * pr], bias[2 + 2 * pr])), hi = cvt_h(addb(acc[mi][3 + 2 * pr], bias[3 + 2 * pr]));
        st16(oHF + (mi * 48 + 5 * j + 2 * wn + pr) * 1024, (u32x4){lo[0], lo[1], hi[0], hi[1]});
      }
#pragma unroll
    for (int mi = 0; mi < 4; ++mi)
      st8(oHF + (mi * 48 + 5 * j + 4) * 1024 + wn * 8, cvt_h(addb(acc[mi][6], bias[6])));
  } else {
    asm volatile("s_waitcnt lgkmcnt(0)" ::: "memory");
    __builtin_amdgcn_s_barrier();
    asm volatile("" ::: "memory");
    char* xk = smem + (wm * 2) * 5 * 1024 + lane * 16;
    char* xv = xk + 20 * 1024;
    if (wn == 0) {
#pragma unroll
      for (int p = 0; p < 2; ++p) {
#pragma unroll
        for (int t = 0; t < 2; ++t) {
          u32x2 lo = cvt_v(acc[2 * p][3 + t]), hi = cvt_v(acc[2 * p + 1][3 + t]);
          *(u32x4*)(xv + (p * 5 + t) * 1024) = (u32x4){lo[0], lo[1], hi[0], hi[1]};
        }
#pragma unroll
        for (int t = 0; t < 3; ++t) {
          u32x2 lo = cvt_q(acc[2 * p][t]), hi = cvt_q(acc[2 * p + 1][t]);
          *(u32x4*)(xk + (p * 5 + t) * 1024) = (u32x4){lo[0], lo[1], hi[0], hi[1]};
        }
      }
    } else {
#pragma unroll
      for (int p = 0; p < 2; ++p) {
#pragma unroll
        for (int t = 0; t < 3; ++t) {
          u32x2 lo = cvt_v(acc[2 * p][2 + t]), hi = cvt_v(acc[2 * p + 1][2 + t]);
          *(u32x4*)(xv + (p * 5 + 2 + t) * 1024) = (u32x4){lo[0], lo[1], hi[0], hi[1]};
        }
#pragma unroll
        for (int t = 0; t < 2; ++t) {
          u32x2 lo = cvt_q(acc[2 * p][t]), hi = cvt_q(acc[2 * p + 1][t]);
          *(u32x4*)(xk + (p * 5 + 3 + t) * 1024) = (u32x4){lo[0], lo[1], hi[0], hi[1]};
        }
      }
    }
    asm volatile("s_waitcnt lgkmcnt(0)" ::: "memory");
    __builtin_amdgcn_s_barrier();
    asm volatile("" ::: "memory");
    const int a0 = w == 0 ? 0 : w - 1;
    bf16x8 pkA[4], vA3[3][4], pkB[4], vB[4];
#pragma unroll
    for (int ks4 = 0; ks4 < 4; ++ks4) {
      pkA[ks4] = *(const bf16x8*)(smem + (ks4 * 5 + w) * 1024 + lane * 16);
#pragma unroll
      for (int t = 0; t < 3; ++t) vA3[t][ks4] = *(const bf16x8*)(smem + (20 + ks4 * 5 + a0 + t) * 1024 + lane * 16);
    }
    const bool hasB = (w == 0 || w == 3);
    const int teB = w == 0 ? 4 : 3;
    if (hasB) {
#pragma unroll
      for (int ks4 = 0; ks4 < 4; ++ks4) {
        pkB[ks4] = *(const bf16x8*)(smem + (ks4 * 5 + 4) * 1024 + lane * 16);
        vB[ks4] = *(const bf16x8*)(smem + (20 + ks4 * 5 + teB) * 1024 + lane * 16);
      }
    }
    if (wn == 0) {
#pragma unroll
      for (int mi = 0; mi < 4; ++mi) {
        u32x2 lo = cvt_h(addb(acc[mi][5], bias[5])), hi = cvt_h(addb(acc[mi][6], bias[6]));
        st16(oHF + (mi * 48 + 40 + j) * 1024, (u32x4){lo[0], lo[1], hi[0], hi[1]});
      }
    } else {
#pragma unroll
      for (int mi = 0; mi < 4; ++mi)
        st8(oQF + (mi * 20 + 16 + (j >> 1)) * 1024 + (j & 1) * 8, cvt_q(acc[mi][5]));
    }
    u16* KVP = (u16*)(ws + OFF_KVP) + (size_t)j * 4 * 6400 + rb * 400;
    u16* ZP = (u16*)(ws + OFF_ZP) + j * 80 * 16 + rb;
    const bf16x8 ones = {0x3f80, 0x3f80, 0x3f80, 0x3f80, 0x3f80, 0x3f80, 0x3f80, 0x3f80};
    auto put_kv = [&](const f32x4& d, int te, int td) {
      const int dg = 16 * td + c, eg = 16 * te + 4 * g;
      const int hd = dg / 20, he = eg / 20;
      if (hd == he) *(u32x2*)(KVP + hd * 6400 + (dg - hd * 20) * 20 + (eg - he * 20)) = cvt_v(d);
    };
    auto put_z = [&](const f32x4& d, int td) {
      if (g == 0) ZP[(16 * td + c) * 16] = (u16)(pack2(d[0], 0.f) & 0xffffu);
    };
    f32x4 dA[4];
#pragma unroll
    for (int t = 0; t < 4; ++t) dA[t] = (f32x4){0.f, 0.f, 0.f, 0.f};
#pragma unroll
    for (int ks4 = 0; ks4 < 4; ++ks4) {
#pragma unroll
      for (int t = 0; t < 3; ++t) dA[t] = MFMA16(vA3[t][ks4], pkA[ks4], dA[t]);
      dA[3] = MFMA16(ones, pkA[ks4], dA[3]);
    }
#pragma unroll
    for (int t = 0; t < 3; ++t) put_kv(dA[t], a0 + t, w);
    put_z(dA[3], w);
    if (hasB) {
      f32x4 dB = {0.f, 0.f, 0.f, 0.f}, dZ = {0.f, 0.f, 0.f, 0.f};
#pragma unroll
      for (int ks4 = 0; ks4 < 4; ++ks4) { dB = MFMA16(vB[ks4], pkB[ks4], dB); dZ = MFMA16(ones, pkB[ks4], dZ); }
      put_kv(dB, teB, 4);
      if (w == 0) put_z(dZ, 4);
    }
  }
}

__global__ __launch_bounds__(256, 2) void k1_kernel(const u16* __restrict__ x, const u16* __restrict__ wqkv,
                                                   const u16* __restrict__ w1, const u16* __restrict__ b1,
                                                   const u16* __restrict__ w2, const u16* __restrict__ gate_w,
                                                   char* __restrict__ ws, const char* outp, const char* wop,
                                                   const char* b2p) {
  extern __shared__ __attribute__((aligned(16))) char smem[];
  const int bid = blockIdx.x;
  const int xcd = bid & 7, idx = bid >> 3;
  const int ri = idx & 7, ci = idx >> 3;
  const int rb = (xcd & 1) * 8 + ri;
  const int cq = (xcd >> 1) * 4 + ci;
  if (bid == 0 && threadIdx.x < 64) ((unsigned*)(ws + OFF_FLAG))[threadIdx.x * 64] = 0u;
  if (idx < 5 && threadIdx.x < 64) {
    const char* pc = (const char*)(__builtin_amdgcn_s_getpc() & ~255ull);
    __builtin_amdgcn_global_load_lds((const unsigned*)(pc + min(idx * 64 + (int)threadIdx.x, 289) * 128), (unsigned*)(smem + K1_LDS), 4, 0, 0);
  }
  if (idx >= 5 && idx < 10 && threadIdx.x < 64) {
    const int t = (idx - 5) * 64 + (int)threadIdx.x;
    constexpr int NWS = (int)((OFF_FLAG + 16384 + 65535) >> 16);
    const char* p = t < NWS ? ws + (size_t)t * 65536
                  : (t < NWS + 40 ? outp + (size_t)(t - NWS) * 65536
                  : (t < NWS + 53 ? wop + (size_t)(t - NWS - 40) * 65536 : b2p));
    __builtin_amdgcn_global_load_lds((const unsigned*)p, (unsigned*)(smem + K1_LDS), 4, 0, 0);
  }
  if (cq & 1) k1_body<false>(smem, rb, cq >> 1, ci, ri, x, wqkv, w1, b1, w2, gate_w, ws);
  else k1_body<true>(smem, rb, cq >> 1, ci, ri, x, wqkv, w1, b1, w2, gate_w, ws);
  asm volatile("s_waitcnt vmcnt(0)" ::: "memory");
}

typedef __attribute__((address_space(1))) unsigned gu32;
#define RLX_AGENT __ATOMIC_RELAXED, __HIP_MEMORY_SCOPE_AGENT
typedef __attribute__((address_space(3))) unsigned lu32;
DI void publish(char* ws, lu32* arrive, int u, int lane) {
  asm volatile("s_waitcnt vmcnt(0)" ::: "memory");
  if (lane == 0) {
    if (__hip_atomic_fetch_add(arrive, 1u, __ATOMIC_RELAXED, __HIP_MEMORY_SCOPE_WORKGROUP) == 1u) {
      __hip_atomic_fetch_add((gu32*)(ws + OFF_FLAG) + (u >> 3) * 64, 1u, RLX_AGENT);
      __hip_atomic_fetch_add((gu32*)(ws + OFF_FLAG) + (32 + (u & 7)) * 64, 1u, RLX_AGENT);
    }
  }
}
DI void task_w(int u, int lane, char* lds  , lu32* arrive, const u16* __restrict__ wo, char* ws) {
  const __amdgpu_buffer_rsrc_t rs = __builtin_amdgcn_make_buffer_rsrc(ws, 0, (int)OFF_FLAG, 0x00020000);
  const int h = u >> 3, cr = u & 7;
  const int g = lane >> 4, c = lane & 15;
  const u16* KVP = (const u16*)(ws + OFF_KVP) + h * 6400 + min(lane, 49) * 8;
  u32x4 kp[16];
#pragma unroll
  for (int p = 0; p < 16; ++p) kp[p] = *(const u32x4*)(KVP + p * 400);
#pragma unroll
  for (int i = 0; i < 4; ++i) {
    const int q = min(i * 64 + lane, 199), row = q / 10, ch = q - row * 10;
    __builtin_amdgcn_global_load_lds((const unsigned*)(wo + (size_t)(h * 20 + row) * DM + cr * 80 + ch * 8),
                                     (unsigned*)(lds + 2048 + i * 1024), 16, 0, 0);
  }
  __builtin_amdgcn_sched_barrier(0);
  __builtin_amdgcn_s_barrier();
  __builtin_amdgcn_sched_barrier(0);
  f32x4 s0 = {0.f, 0.f, 0.f, 0.f}, s1 = {0.f, 0.f, 0.f, 0.f};
#pragma unroll
  for (int p = 0; p < 16; ++p) {
    s0 += (f32x4){bf_lo(kp[p][0]), bf_hi(kp[p][0]), bf_lo(kp[p][1]), bf_hi(kp[p][1])};
    s1 += (f32x4){bf_lo(kp[p][2]), bf_hi(kp[p][2]), bf_lo(kp[p][3]), bf_hi(kp[p][3])};
  }
  asm volatile("s_waitcnt vmcnt(0)" ::: "memory");
  bf16x8 wf[5];
  {
    const int r0 = min(8 * g + (c >> 2), 19), r1 = min(8 * g + 4 + (c >> 2), 19);
#pragma unroll
    for (int t = 0; t < 5; ++t) {
      s16x4 lo = __builtin_amdgcn_ds_read_tr16_b64_v4i16((lds_s16x4_ptr)(lds + 2048 + r0 * 160 + t * 32 + (c & 3) * 8));
      s16x4 hi = __builtin_amdgcn_ds_read_tr16_b64_v4i16((lds_s16x4_ptr)(lds + 2048 + r1 * 160 + t * 32 + (c & 3) * 8));
      wf[t] = __builtin_shufflevector(lo, hi, 0, 1, 2, 3, 4, 5, 6, 7);
    }
  }
  *(u32x4*)(lds + lane * 32) = (u32x4){0u, 0u, 0u, 0u};
  *(u32x4*)(lds + lane * 32 + 16) = (u32x4){0u, 0u, 0u, 0u};
  __builtin_amdgcn_s_waitcnt(0xC07F);
  if (lane < 50) {
    const int i0 = lane * 8, d0 = i0 / 20, e0 = i0 - d0 * 20;
    *(u32x2*)(lds + d0 * 64 + e0 * 2) = (u32x2){pack2(s0[0], s0[1]), pack2(s0[2], s0[3])};
    const int i1 = i0 + 4, d1 = i1 / 20, e1 = i1 - d1 * 20;
    *(u32x2*)(lds + d1 * 64 + e1 * 2) = (u32x2){pack2(s1[0], s1[1]), pack2(s1[2], s1[3])};
  }
  __builtin_amdgcn_s_waitcnt(0xC07F);
  bf16x8 ka0 = *(const bf16x8*)(lds + c * 64 + g * 16);
  bf16x8 ka1 = *(const bf16x8*)(lds + (16 + c) * 64 + g * 16);
#pragma unroll
  for (int t = 0; t < 5; ++t) {
    f32x4 d0 = MFMA16(ka0, wf[t], ((f32x4){0.f, 0.f, 0.f, 0.f}));
    f32x4 d1 = MFMA16(ka1, wf[t], ((f32x4){0.f, 0.f, 0.f, 0.f}));
    {
      const int k = h * 20 + 4 * g;
      const int kb = k >> 5, half = (k >> 4) & 1, gg = (k & 15) >> 2;
      __builtin_amdgcn_raw_buffer_store_b64((u32x2){pack2(d0[0], d0[1]), pack2(d0[2], d0[3])}, rs,
          (int)OFF_WPF + (kb * 40 + cr * 5 + t) * 1024 + half * 512 + (gg * 16 + c) * 8, 0, 16);
    }
    if (g == 0) {
      const int k = h * 20 + 16;
      const int kb = k >> 5, half = (k >> 4) & 1, gg = (k & 15) >> 2;
      __builtin_amdgcn_raw_buffer_store_b64((u32x2){pack2(d1[0], d1[1]), pack2(d1[2], d1[3])}, rs,
          (int)OFF_WPF + (kb * 40 + cr * 5 + t) * 1024 + half * 512 + (gg * 16 + c) * 8, 0, 16);
    }
  }
  publish(ws, arrive, u, lane);
}

DI void task_a(int u, int lane, char* lds  , lu32* arrive, char* ws) {
  const __amdgpu_buffer_rsrc_t rs = __builtin_amdgcn_make_buffer_rsrc(ws, 0, (int)OFF_FLAG, 0x00020000);
  const int rt = u >> 1, kh = u & 1;
  const int g = lane >> 4, c = lane & 15;
  const u16* ZP = (const u16*)(ws + OFF_ZP) + kh * 320 * 16;
  u32x4 zp[10];
#pragma unroll
  for (int i = 0; i < 10; ++i) zp[i] = *(const u32x4*)(ZP + i * 512 + lane * 8);
  const char* QF = ws + OFF_QF + ((size_t)rt * 20 + kh * 10) * 1024;
  u32x4 qf[10];
#pragma unroll
  for (int b = 0; b < 10; ++b) qf[b] = *(const u32x4*)(QF + b * 1024 + lane * 16);
  const float gate = ((const float*)(ws + OFF_G))[rt * 16 + c];
  __builtin_amdgcn_sched_barrier(0);
  __builtin_amdgcn_s_barrier();
  __builtin_amdgcn_sched_barrier(0);
#pragma unroll
  for (int i = 0; i < 10; ++i) {
    float t = ((bf_lo(zp[i][0]) + bf_hi(zp[i][0])) + (bf_lo(zp[i][1]) + bf_hi(zp[i][1]))) +
              ((bf_lo(zp[i][2]) + bf_hi(zp[i][2])) + (bf_lo(zp[i][3]) + bf_hi(zp[i][3])));
    t += __builtin_bit_cast(float, __builtin_amdgcn_update_dpp(0, __builtin_bit_cast(int, t), 0xB1, 0xF, 0xF, true));
    if ((lane & 1) == 0) *(u16*)(lds + (i * 32 + (lane >> 1)) * 2) = (u16)(pack2(t, 0.f) & 0xffffu);
  }
  __builtin_amdgcn_s_waitcnt(0xC07F);
  f32x4 den = {0.f, 0.f, 0.f, 0.f};
#pragma unroll
  for (int b = 0; b < 10; ++b) {
    const int k0 = 32 * b + 4 * g, k1 = k0 + 16;
    u32x2 zl = *(const u32x2*)(lds + k0 * 2), zh = *(const u32x2*)(lds + k1 * 2);
    const bool m0 = (k0 / 20) == c, m1 = (k1 / 20) == c;
    u32x4 zf = {m0 ? zl[0] : 0u, m0 ? zl[1] : 0u, m1 ? zh[0] : 0u, m1 ? zh[1] : 0u};
    den = MFMA16(__builtin_bit_cast(bf16x8, zf), __builtin_bit_cast(bf16x8, qf[b]), den);
  }
  f32x4 rd;
#pragma unroll
  for (int r = 0; r < 4; ++r) rd[r] = gate / (den[r] + 1e-6f);
  *(f32x4*)(lds + 1024 + c * 64 + g * 16) = rd;
  __builtin_amdgcn_s_waitcnt(0xC07F);
#pragma unroll
  for (int b = 0; b < 10; ++b) {
    const int k0 = 32 * b + 4 * g, k1 = k0 + 16;
    const float r0 = *(const float*)(lds + 1024 + c * 64 + (k0 / 20) * 4);
    const float r1 = *(const float*)(lds + 1024 + c * 64 + (k1 / 20) * 4);
    u32x4 o;
    o[0] = pack2(bf_lo(qf[b][0]) * r0, bf_hi(qf[b][0]) * r0);
    o[1] = pack2(bf_lo(qf[b][1]) * r0, bf_hi(qf[b][1]) * r0);
    o[2] = pack2(bf_lo(qf[b][2]) * r1, bf_hi(qf[b][2]) * r1);
    o[3] = pack2(bf_lo(qf[b][3]) * r1, bf_hi(qf[b][3]) * r1);
    __builtin_amdgcn_raw_buffer_store_b128(o, rs, (int)OFF_AF + (rt * 20 + kh * 10 + b) * 1024 + lane * 16, 0, 16);
  }
  publish(ws, arrive, u, lane);
}

constexpr int K2_DEPTH = 2;
constexpr int K2GO = 6;
__global__ __launch_bounds__(384, 2) void k2_kernel(const u16* __restrict__ wo, const u16* __restrict__ b2,
                                                   char* ws, u16* __restrict__ out) {
  __shared__ __attribute__((aligned(16))) char red[4 * 20 * 1024 + 8192 + 16];
  lu32* ready = (lu32*)(red + 81920 + 8192);
  const int bid = blockIdx.x;
  const int xcd = bid & 7, idx = bid >> 3;
  const int rb = (xcd >> 1) * 8 + (idx & 7);
  const int cb = (xcd & 1) * 4 + (idx >> 3);
  const int lane = threadIdx.x & 63;
  const int w = __builtin_amdgcn_readfirstlane(threadIdx.x >> 6);
  const int g = lane >> 4, c = lane & 15;

  if (w >= 4) {
    __builtin_amdgcn_s_setprio(3);
    if (w == 4) {
      if (lane < 3) __hip_atomic_store(ready + lane, 0u, __ATOMIC_RELAXED, __HIP_MEMORY_SCOPE_WORKGROUP);
      asm volatile("s_waitcnt lgkmcnt(0)" ::: "memory");
      task_w(bid, lane, red + 81920, ready + 2, wo, ws);
    } else {
      task_a(bid, lane, red + 81920 + 6144, ready + 2, ws);
      gu32* cntA = (gu32*)(ws + OFF_FLAG) + rb * 64;
      gu32* cntW = (gu32*)(ws + OFF_FLAG) + (32 + cb) * 64;
      for (unsigned spins = 0; spins < (1u << 20); ++spins) {
        if (__builtin_amdgcn_readfirstlane(__hip_atomic_load(ready + 1, __ATOMIC_RELAXED, __HIP_MEMORY_SCOPE_WORKGROUP)) != 0u) break;
        __builtin_amdgcn_s_sleep(2);
      }
      for (unsigned spins = 0; spins < (1u << 16); ++spins) {
        const unsigned a = __hip_atomic_load(cntA, RLX_AGENT), b = __hip_atomic_load(cntW, RLX_AGENT);
        if (__builtin_amdgcn_readfirstlane(a) == 8u && __builtin_amdgcn_readfirstlane(b) == 32u) break;
        __builtin_amdgcn_s_sleep(4);
      }
      if (lane == 0) __hip_atomic_store(ready, 1u, __ATOMIC_RELAXED, __HIP_MEMORY_SCOPE_WORKGROUP);
    }
    return;
  }

  const __amdgpu_buffer_rsrc_t rs = __builtin_amdgcn_make_buffer_rsrc(ws, 0, (int)OFF_FLAG, 0x00020000);
  const int voff = lane * 16;
  const int oH = (int)OFF_HF + (rb * 4 * 48 + w) * 1024;
  const int oA = (int)OFF_AF + (rb * 4 * 20 + w) * 1024;
  const int oW2 = (int)OFF_W2F + (w * 40 + cb * 5) * 1024;
  const int oWP = (int)OFF_WPF + (w * 40 + cb * 5) * 1024;

  f32x4 acc[4][5];
#pragma unroll
  for (int mi = 0; mi < 4; ++mi)
#pragma unroll
    for (int ni = 0; ni < 5; ++ni) acc[mi][ni] = (f32x4){0.f, 0.f, 0.f, 0.f};

  constexpr int NS = K2_DEPTH + 1;
  u32x4 fa[NS][4], fb[NS][5];
  float gv[4];
  u32x2 b2v[5];
  auto load = [&](int i, int slot) {
    if (i < 12) {
#pragma unroll
      for (int ni = 0; ni < 5; ++ni) fb[slot][ni] = __builtin_amdgcn_raw_buffer_load_b128(rs, voff, oW2 + (4 * i * 40 + ni) * 1024, 0);
#pragma unroll
      for (int mi = 0; mi < 4; ++mi) fa[slot][mi] = __builtin_amdgcn_raw_buffer_load_b128(rs, voff, oH + (mi * 48 + 4 * i) * 1024, 0);
    } else {
      const int ii = i - 12;
#pragma unroll
      for (int ni = 0; ni < 5; ++ni) {
        u32x2 lo = __builtin_amdgcn_raw_buffer_load_b64(rs, lane * 8, oWP + (4 * ii * 40 + ni) * 1024, 16);
        u32x2 hi = __builtin_amdgcn_raw_buffer_load_b64(rs, lane * 8, oWP + (4 * ii * 40 + ni) * 1024 + 512, 16);
        fb[slot][ni] = (u32x4){lo[0], lo[1], hi[0], hi[1]};
      }
#pragma unroll
      for (int mi = 0; mi < 4; ++mi) fa[slot][mi] = __builtin_amdgcn_raw_buffer_load_b128(rs, voff, oA + (mi * 20 + 4 * ii) * 1024, 16);
    }
  };
  __builtin_amdgcn_s_barrier();
  __builtin_amdgcn_sched_barrier(0);
#pragma unroll
  for (int d = 0; d < K2_DEPTH; ++d) load(d, d);
#pragma unroll
  for (int mi = 0; mi < 4; ++mi)
    gv[mi] = __uint_as_float(__builtin_amdgcn_raw_buffer_load_b32(rs, c * 4, (int)OFF_G + (rb * 64 + mi * 16) * 4, 0));
  __builtin_amdgcn_sched_barrier(0);
#pragma unroll
  for (int i = 0; i < 17; ++i) {
    const int slot = i % NS;
    if (i == K2GO) {
      if (lane == 0) __hip_atomic_store(ready + 1, 1u, __ATOMIC_RELAXED, __HIP_MEMORY_SCOPE_WORKGROUP);
      __builtin_amdgcn_sched_barrier(0);
    }
    if (i == 12 - K2_DEPTH) {
      for (unsigned spins = 0; spins < (1u << 20); ++spins) {
        if (__builtin_amdgcn_readfirstlane(__hip_atomic_load(ready, __ATOMIC_RELAXED, __HIP_MEMORY_SCOPE_WORKGROUP)) != 0u) break;
        __builtin_amdgcn_s_sleep(1);
      }
      asm volatile("" ::: "memory");
      __builtin_amdgcn_sched_barrier(0);
    }
    if (i == 12) {
#pragma unroll
      for (int mi = 0; mi < 4; ++mi) {
        gv[mi] = 1.0f - gv[mi];
#pragma unroll
        for (int ni = 0; ni < 5; ++ni) acc[mi][ni] *= gv[mi];
      }
      __builtin_amdgcn_sched_barrier(0);
    }
    if (i + K2_DEPTH < 17) load(i + K2_DEPTH, (i + K2_DEPTH) % NS);
    if (i == 15) {
#pragma unroll
      for (int ni = 0; ni < 5; ++ni) b2v[ni] = *(const u32x2*)(b2 + cb * 80 + ni * 16 + 4 * g);
      __builtin_amdgcn_sched_barrier(0);
    }
#pragma unroll
    for (int mi = 0; mi < 4; ++mi)
#pragma unroll
      for (int ni = 0; ni < 5; ++ni)
        acc[mi][ni] = MFMA16(__builtin_bit_cast(bf16x8, fb[slot][ni]), __builtin_bit_cast(bf16x8, fa[slot][mi]), acc[mi][ni]);
    if (i + K2_DEPTH < 12) {
#pragma unroll
      for (int k = 0; k < 9; ++k) {
        __builtin_amdgcn_sched_group_barrier(0x008, 2, 0);
        __builtin_amdgcn_sched_group_barrier(0x020, 1, 0);
      }
      __builtin_amdgcn_sched_group_barrier(0x008, 2, 0);
    } else if (i + K2_DEPTH < 17) {
#pragma unroll
      for (int k = 0; k < 14; ++k) {
        __builtin_amdgcn_sched_group_barrier(0x008, 1, 0);
        __builtin_amdgcn_sched_group_barrier(0x020, 1, 0);
      }
      __builtin_amdgcn_sched_group_barrier(0x008, 6, 0);
    }
    __builtin_amdgcn_sched_barrier(0);
  }

#pragma unroll
  for (int mi = 0; mi < 4; ++mi)
    if (mi != w) {
#pragma unroll
      for (int ni = 0; ni < 5; ++ni) *(f32x4*)(red + ((w * 4 + mi) * 5 + ni) * 1024 + lane * 16) = acc[mi][ni];
    }
  __syncthreads();
  float gw = gv[0];
  f32x4 mine[5];
#pragma unroll
  for (int ni = 0; ni < 5; ++ni) mine[ni] = acc[0][ni];
#pragma unroll
  for (int mi = 1; mi < 4; ++mi)
    if (w == mi) {
      gw = gv[mi];
#pragma unroll
      for (int ni = 0; ni < 5; ++ni) mine[ni] = acc[mi][ni];
    }
#pragma unroll
  for (int ni = 0; ni < 5; ++ni) {
    f32x4 s = mine[ni];
#pragma unroll
    for (int o = 1; o < 4; ++o) {
      const int ow = (w + o) & 3;
      s += *(const f32x4*)(red + ((ow * 4 + w) * 5 + ni) * 1024 + lane * 16);
    }
    const int n = cb * 80 + ni * 16 + 4 * g;
    const u32x2 bv = b2v[ni];
    s[0] += gw * bf_lo(bv[0]); s[1] += gw * bf_hi(bv[0]); s[2] += gw * bf_lo(bv[1]); s[3] += gw * bf_hi(bv[1]);
    *(u32x2*)(out + (size_t)(rb * 64 + w * 16 + c) * DM + n) = (u32x2){pack2(s[0], s[1]), pack2(s[2], s[3])};
  }
}

extern "C" void kernel_launch(void* const* d_in, const int* in_sizes, int n_in, void* d_out, int out_size,
                              void* d_ws, size_t ws_size, hipStream_t stream) {
  const u16* x = (const u16*)d_in[0];
  const u16* wqkv = (const u16*)d_in[1];
  const u16* wo = (const u16*)d_in[2];
  const u16* w1 = (const u16*)d_in[4];
  const u16* b1 = (const u16*)d_in[5];
  const u16* w2 = (const u16*)d_in[6];
  const u16* b2 = (const u16*)d_in[7];
  const u16* gw = (const u16*)d_in[8];
  char* ws = (char*)d_ws;
  (void)hipFuncSetAttribute((const void*)k1_kernel, hipFuncAttributeMaxDynamicSharedMemorySize, K1_LDS + 14336);
  k1_kernel<<<256, 256, K1_LDS + 14336, stream>>>(x, wqkv, w1, b1, w2, gw, ws, (const char*)d_out, (const char*)wo, (const char*)b2);
  k2_kernel<<<256, 384, 0, stream>>>(wo, b2, ws, (u16*)d_out);
}
```
